```python
import torch

def _softmax(x, axis=-1):
    return torch.softmax(x, dim=axis)

def _gelu(x):
    return torch.nn.functional.gelu(x, approximate='tanh')

def _silu(x):
    return torch.nn.functional.silu(x)

def _sigmoid(x):
    return torch.sigmoid(x)

def _layernorm(x, axis=-1, eps=1e-05):
    m = x.mean(dim=axis, keepdim=True)
    v = x.var(dim=axis, keepdim=True, unbiased=False)
    return (x - m) / torch.sqrt(v + eps)

def _scatter_add(tgt, idx, src, axis=0):
    return tgt.clone().index_add_(axis, idx, src)

def _segment_sum(src, seg, n):
    out = torch.zeros((n,) + src.shape[1:], dtype=src.dtype)
    return out.index_add_(0, seg, src)

def _pad(x, pw):
    flat = tuple((v for pair in reversed(pw) for v in pair))
    return torch.nn.functional.pad(x, flat)
kernel_name = 'standalone__mixer_gmlp__prompt'

def setup_inputs():

    _w = lambda a: (lambda t: t.to(torch.bfloat16) if t.is_floating_point() else t)(torch.as_tensor(a))
    B, S, D, F = (1, 2048, 576, 4608)
    Fh = F // 2
    return {'x': _w((lambda *s: torch.randn(*s, dtype=torch.bfloat16) * 0.05)(B, S, D)), 'w_in': _w((lambda *s: torch.randn(*s, dtype=torch.bfloat16) * 0.4)(D, 2 * Fh)), 'w_sp': _w(0.01 * (lambda *s: torch.randn(*s, dtype=torch.bfloat16) * 0.4)(S, S)), 'b_sp': _w(1.0 + 0.01 * (lambda *s: torch.randn(*s, dtype=torch.bfloat16) * 0.05)(S)), 'w_out': _w((lambda *s: torch.randn(*s, dtype=torch.bfloat16) * 0.05)(Fh, D))}

def reference(x, w_in, w_sp, b_sp, w_out):
    B, S, D = x.shape
    F = w_in.shape[-1]
    Fh = F // 2
    z = _gelu(torch.einsum('bsd,df->bsf', x, w_in))
    z1 = z[..., :Fh].clone()
    z2 = _layernorm(z[..., Fh:], axis=-1)
    g = torch.einsum('ts,bsf->btf', w_sp, z2) + b_sp[None, :, None]
    return torch.einsum('bsf,fd->bsd', z1 * g, w_out)
```

```cpp
#include <hip/hip_runtime.h>
#include <hip/hip_bf16.h>
#include <stdint.h>
typedef unsigned short u16;
typedef short bf16x8 __attribute__((ext_vector_type(8)));
typedef float f32x4 __attribute__((ext_vector_type(4)));

__device__ __forceinline__ float bf2f(u16 b) { return __uint_as_float(((unsigned)b) << 16); }
__device__ __forceinline__ u16 f2bf(float f) { __hip_bfloat16 h = __float2bfloat16(f); return *reinterpret_cast<u16*>(&h); }
__device__ __forceinline__ float rbf(float f) { return bf2f(f2bf(f)); }
__device__ __forceinline__ float gelu_tanh(float x) {
    float u = 0.7978845608028654f * (x + 0.044715f * x * x * x);
    return 0.5f * x * (1.0f + tanhf(u));
}

template <int MODE>
__global__ __launch_bounds__(256) void gemm_k(const u16* __restrict__ A, int lda, const u16* __restrict__ B, int ldb,
                                              u16* __restrict__ C, int ldc, int K,
                                              const u16* __restrict__ z, int ldz, const u16* __restrict__ bias) {
    __shared__ u16 sA[64][40];
    __shared__ u16 sB[64][40];
    int tid = threadIdx.x, wid = tid >> 6, lane = tid & 63;
    int bm = blockIdx.y * 64, bn = blockIdx.x * 64;
    int wr = wid >> 1, wc = wid & 1;
    f32x4 acc[2][2] = {};
    for (int k0 = 0; k0 < K; k0 += 32) {
        {
            int r = tid >> 2, c = (tid & 3) * 8;
            *(uint4*)&sA[r][c] = *(const uint4*)&A[(size_t)(bm + r) * lda + k0 + c];
            int kr = tid >> 3, nc = (tid & 7) * 8;
            uint4 v = *(const uint4*)&B[(size_t)(k0 + kr) * ldb + bn + nc];
            const u16* pv = (const u16*)&v;
            for (int j = 0; j < 8; ++j) sB[nc + j][kr] = pv[j];
        }
        __syncthreads();
        bf16x8 a[2], b[2];
        for (int m = 0; m < 2; ++m) a[m] = *(const bf16x8*)&sA[wr * 32 + m * 16 + (lane & 15)][(lane >> 4) * 8];
        for (int n = 0; n < 2; ++n) b[n] = *(const bf16x8*)&sB[wc * 32 + n * 16 + (lane & 15)][(lane >> 4) * 8];
        for (int m = 0; m < 2; ++m)
            for (int n = 0; n < 2; ++n) acc[m][n] = __builtin_amdgcn_mfma_f32_16x16x32_bf16(a[m], b[n], acc[m][n], 0, 0, 0);
        __syncthreads();
    }
    for (int m = 0; m < 2; ++m)
        for (int n = 0; n < 2; ++n)
            for (int j = 0; j < 4; ++j) {
                int r = bm + wr * 32 + m * 16 + (lane >> 4) * 4 + j, c = bn + wc * 32 + n * 16 + (lane & 15);
                float v = rbf(acc[m][n][j]);
                if (MODE == 0) v = gelu_tanh(v);
                if (MODE == 1) { v = rbf(v + bf2f(bias[r])); v = bf2f(z[(size_t)r * ldz + c]) * v; }
                C[(size_t)r * ldc + c] = f2bf(v);
            }
}

__global__ __launch_bounds__(256) void ln_k(const u16* __restrict__ z, int ldz, u16* __restrict__ z2, int n) {
    __shared__ float red[8];
    int row = blockIdx.x, tid = threadIdx.x;
    const u16* p = z + (size_t)row * ldz;
    float s = 0;
    for (int i = tid; i < n; i += 256) s += bf2f(p[i]);
    for (int o = 32; o; o >>= 1) s += __shfl_xor(s, o);
    if ((tid & 63) == 0) red[tid >> 6] = s;
    __syncthreads();
    float mean = (red[0] + red[1] + red[2] + red[3]) / n;
    float q = 0;
    for (int i = tid; i < n; i += 256) { float d = bf2f(p[i]) - mean; q += d * d; }
    for (int o = 32; o; o >>= 1) q += __shfl_xor(q, o);
    if ((tid & 63) == 0) red[4 + (tid >> 6)] = q;
    __syncthreads();
    float var = (red[4] + red[5] + red[6] + red[7]) / n;
    float mb = rbf(mean), vb = rbf(var);
    float sd = rbf(sqrtf(rbf(vb + 1e-5f)));
    for (int i = tid; i < n; i += 256) z2[(size_t)row * n + i] = f2bf(rbf(bf2f(p[i]) - mb) / sd);
}

extern "C" void kernel_launch(void* const* d_in, const int* in_sizes, int n_in, void* d_out, int out_size, void* d_ws, size_t ws_size,
                              hipStream_t stream) {
    const u16* x = (const u16*)d_in[0];
    const u16* w_in = (const u16*)d_in[1];
    const u16* w_sp = (const u16*)d_in[2];
    const u16* b_sp = (const u16*)d_in[3];
    const u16* w_out = (const u16*)d_in[4];
    u16* out = (u16*)d_out;
    const int S = 2048, D = 576, F = 4608, Fh = 2304;
    u16* z = (u16*)d_ws;
    u16* z2 = z + (size_t)S * F;
    u16* u = z2 + (size_t)S * Fh;
    gemm_k<0><<<dim3(F / 64, S / 64), 256, 0, stream>>>(x, D, w_in, F, z, F, D, nullptr, 0, nullptr);
    ln_k<<<S, 256, 0, stream>>>(z + Fh, F, z2, Fh);
    gemm_k<1><<<dim3(Fh / 64, S / 64), 256, 0, stream>>>(w_sp, S, z2, Fh, u, Fh, S, z, F, b_sp);
    gemm_k<2><<<dim3(D / 64, S / 64), 256, 0, stream>>>(u, Fh, w_out, D, out, D, Fh, nullptr, 0, nullptr);
}
```

```cpp
#include <hip/hip_runtime.h>
#include <hip/hip_bf16.h>
#include <stdint.h>

typedef unsigned short u16;
typedef short s16x4 __attribute__((ext_vector_type(4)));
typedef short s16x8 __attribute__((ext_vector_type(8)));
typedef __bf16 bf16x2 __attribute__((ext_vector_type(2)));
typedef __bf16 bf16x8 __attribute__((ext_vector_type(8)));
typedef float f32x2 __attribute__((ext_vector_type(2)));
typedef float f32x4 __attribute__((ext_vector_type(4)));
typedef unsigned u32x2 __attribute__((ext_vector_type(2)));
typedef unsigned u32x4 __attribute__((ext_vector_type(4)));
typedef int i32x4 __attribute__((ext_vector_type(4)));
#define LAS __attribute__((address_space(3)))
#define DEVI __device__ __forceinline__

constexpr int S = 2048, D = 576, F = 4608, FH = 2304;
constexpr int BLK = 1056;
constexpr int AP = 16 * BLK;
constexpr int AREM = AP + 16 * BLK;
constexpr int SLOT = AREM + 2048;
constexpr int MISC = 4 * SLOT;
constexpr int LDS_BYTES = 163840;
constexpr unsigned WS_EPOCH = 0;
constexpr unsigned WS_ZFLAG = 4096;
constexpr unsigned WS_UFLAG = 8192;
constexpr unsigned WS_STATS = 16384;
constexpr unsigned WS_Z2T = 1u << 20;
constexpr unsigned WS_U = WS_Z2T + 16u * 64 * 9216;
constexpr unsigned WS_WO = WS_U + 512u * 18432;
constexpr unsigned WS_END = WS_WO + 256u * 20736;
constexpr unsigned WS_DBG = 32u << 20;

template <int N> struct IC { static constexpr int v = N; };
template <int B, int E, class Fn> DEVI void sfor(Fn&& f) { if constexpr (B < E) { f(IC<B>{}); sfor<B + 1, E>(f); } }

DEVI i32x4 make_srd(const void* p, unsigned bytes) {
    i32x4 r; const unsigned long long a = (unsigned long long)p;
    r.x = (int)(unsigned)a; r.y = (int)((unsigned)(a >> 32) & 0xffffu); r.z = (int)bytes; r.w = 0x00020000;
    return r;
}
DEVI void dma16(i32x4 srd, unsigned voff, unsigned soff, unsigned lds) {
    asm volatile("s_mov_b32 m0, %3\n\ts_nop 0\n\tbuffer_load_dwordx4 %0, %1, %2 offen lds" ::"v"(voff), "s"(srd), "s"(soff), "s"(lds) : "memory");
}
DEVI void dma16_sc1(i32x4 srd, unsigned voff, unsigned soff, unsigned lds) {
    asm volatile("s_mov_b32 m0, %3\n\ts_nop 0\n\tbuffer_load_dwordx4 %0, %1, %2 offen sc1 lds" ::"v"(voff), "s"(srd), "s"(soff), "s"(lds) : "memory");
}
DEVI void dma16x4_sc1(i32x4 srd, unsigned voff, unsigned soff, unsigned lds) {
    asm volatile("s_mov_b32 m0, %3\n\ts_nop 0\n\tbuffer_load_dwordx4 %0, %1, %2 offen sc1 lds\n\t"
                 "buffer_load_dwordx4 %0, %1, %2 offen offset:1024 sc1 lds\n\t"
                 "buffer_load_dwordx4 %0, %1, %2 offen offset:2048 sc1 lds\n\t"
                 "buffer_load_dwordx4 %0, %1, %2 offen offset:3072 sc1 lds" ::"v"(voff), "s"(srd), "s"(soff), "s"(lds) : "memory");
}
DEVI void st16_sc1(i32x4 srd, unsigned voff, unsigned soff, u32x4 v) {
    asm volatile("buffer_store_dwordx4 %0, %1, %2, %3 offen sc1\n\ts_nop 1" ::"v"(v), "v"(voff), "s"(srd), "s"(soff) : "memory");
}
DEVI void st8_sc1(i32x4 srd, unsigned voff, unsigned soff, u32x2 v) {
    asm volatile("buffer_store_dwordx2 %0, %1, %2, %3 offen sc1\n\ts_nop 1" ::"v"(v), "v"(voff), "s"(srd), "s"(soff) : "memory");
}
DEVI void st4_sc1(i32x4 srd, unsigned voff, unsigned soff, unsigned v) {
    asm volatile("buffer_store_dword %0, %1, %2, %3 offen sc1\n\ts_nop 1" ::"v"(v), "v"(voff), "s"(srd), "s"(soff) : "memory");
}
template <int N> DEVI void waitv() { asm volatile("s_waitcnt vmcnt(%0)" ::"i"(N) : "memory"); }
template <int NA, int NB> DEVI void waitv2(bool first) { if (first) waitv<NA>(); else waitv<NB>(); }
DEVI void waitl0() { asm volatile("s_waitcnt lgkmcnt(0)" ::: "memory"); }
DEVI void bar() { asm volatile("s_barrier" ::: "memory"); }
#define SCHED0() __builtin_amdgcn_sched_barrier(0)

DEVI unsigned pk_bf16(float lo, float hi) { f32x2 x = {lo, hi}; return __builtin_bit_cast(unsigned, __builtin_convertvector(x, bf16x2)); }
DEVI float bf_lo(unsigned p) { return __uint_as_float(p << 16); }
DEVI float bf_hi(unsigned p) { return __uint_as_float(p & 0xffff0000u); }
DEVI float rbf(float x) { return bf_lo(pk_bf16(x, x)); }
DEVI f32x2 gelu2(f32x2 h) {
    const float c1 = -2.302208198f, c2 = -0.1029432397f;
    f32x2 t = (h * h) * c2 + c1;
    t = t * h;
    f32x2 e = {__builtin_amdgcn_exp2f(t.x), __builtin_amdgcn_exp2f(t.y)};
    e = e + 1.0f;
    f32x2 r = {__builtin_amdgcn_rcpf(e.x), __builtin_amdgcn_rcpf(e.y)};
    return h * r;
}
DEVI unsigned gelu_pk(unsigned hp) { f32x2 z = gelu2((f32x2){bf_lo(hp), bf_hi(hp)}); return pk_bf16(z.x, z.y); }
template <int CTRL> DEVI float dpp_add(float x) {
    return x + __int_as_float(__builtin_amdgcn_update_dpp(0, __float_as_int(x), CTRL, 0xf, 0xf, false));
}
DEVI float row16_sum(float x) { x = dpp_add<0x128>(x); x = dpp_add<0x124>(x); x = dpp_add<0x122>(x); x = dpp_add<0x121>(x); return x; }
DEVI f32x4 mfma32(s16x8 a, s16x8 b, f32x4 c) {
    return __builtin_amdgcn_mfma_f32_16x16x32_bf16(__builtin_bit_cast(bf16x8, a), __builtin_bit_cast(bf16x8, b), c, 0, 0, 0);
}
DEVI s16x8 lds_b128(const LAS char* p) { return *(const LAS s16x8*)p; }
DEVI s16x4 lds_tr(const LAS char* p) { return __builtin_amdgcn_ds_read_tr16_b64_v4i16((LAS s16x4*)p); }
DEVI s16x8 cat8(s16x4 lo, s16x4 hi) { return __builtin_shufflevector(lo, hi, 0, 1, 2, 3, 4, 5, 6, 7); }
DEVI int colmap8(int j) { return 576 * (j >> 2) + 512 + 16 * (j & 3); }
DEVI int colmap0(int j) { return 576 * (j >> 2) + 128 * (j & 3); }
DEVI int fresh_lane() { int l; asm volatile("v_mbcnt_lo_u32_b32 %0, -1, 0\n\tv_mbcnt_hi_u32_b32 %0, -1, %0" : "=v"(l)); return l; }
template <int DLT> DEVI void bump(unsigned& a) { asm volatile("v_add_u32 %0, %1, %0" : "+v"(a) : "i"(DLT)); }
DEVI const LAS char* lp(unsigned a) { return (const LAS char*)(size_t)a; }
DEVI unsigned addrB(unsigned lds0, int w, int lane) { const int q = lane >> 4, c = lane & 15; return lds0 + (4 * w + (c & 3)) * BLK + ((c >> 3) + 2 * ((c >> 2) & 1)) * 128 + q * 16; }
DEVI LAS char* stash_at(LAS char* lds, int w, int lane, int n) {
    return n < 4 ? lds + (4 * w + n) * BLK + lane * 16 : n < 8 ? lds + SLOT + (4 * w + n - 4) * BLK + lane * 16 : lds + MISC + w * 1024 + lane * 16;
}
constexpr unsigned long long SPIN_TICKS = 400000ull;

#define STAMP(k) do {} while (0)

__global__ __launch_bounds__(512, 2) void gmlp_kernel(const u16* __restrict__ x, const u16* __restrict__ w_in, const u16* __restrict__ w_sp,
                                                      const u16* __restrict__ b_sp, const u16* __restrict__ w_out, u16* __restrict__ out,
                                                      char* __restrict__ ws) {
    extern __shared__ __attribute__((aligned(16))) char shm_[];
    LAS char* const lds = (LAS char*)shm_;
    const unsigned lds0 = (unsigned)(size_t)lds;
    const int wid = __builtin_amdgcn_readfirstlane(threadIdx.x >> 6);
    const int xcd = blockIdx.x & 7, loc = blockIdx.x >> 3;
    const int pi = 4 * (xcd >> 1) + (loc & 3), pj = 8 * (xcd & 1) + (loc >> 2);
    const unsigned tag = *(const unsigned*)(ws + WS_EPOCH) + 1u;
    const i32x4 swc = make_srd(ws, WS_END);
    STAMP(0);

    if (wid < 4) {
        const int w = wid;
        f32x4 acc[9][2];
        {
            unsigned aB, aM, aR;
            {
                const int lane = fresh_lane(), q = lane >> 4, qq = (lane >> 2) & 3, pp = lane & 3;
                aB = addrB(lds0, w, lane);
                aM = lds0 + AP + qq * BLK + ((q & 1) + 4 * (q >> 1)) * 128 + pp * 8;
                aR = lds0 + AREM + (qq + 4 * (q & 1) + 16 * (q >> 1)) * 32 + pp * 8;
            }
            s16x8 X[18][2];
            constexpr int WIN = 5;
            s16x8 Wf[WIN];
            auto pass = [&](auto PA) {
                constexpr bool first = PA.v == 0;
                constexpr int T0 = first ? 0 : 9;
                auto rd = [&](auto N) {
                    constexpr int n = N.v, kt = n / 18, ks = (n % 18) / 9, a = n % 9;
                    if constexpr (n % 18 == 0) {
                        SCHED0(); bar();
                        if constexpr (T0 + kt > 0) {
                            constexpr int dlt = ((T0 + kt) & 3) == 0 ? -3 * SLOT : SLOT;
                            if constexpr (first) bump<dlt>(aB);
                            bump<dlt>(aM); bump<dlt>(aR);
                        }
                        SCHED0();
                    }
                    if constexpr (first && a == 0) {
                        X[2 * kt + ks][0] = lds_b128(lp(aB) + ks * 64);
                        X[2 * kt + ks][1] = lds_b128(lp(aB) + ks * 64 + 512);
                    }
                    if constexpr (a < 8) {
                        constexpr int o = (a >> 2) * 8 * BLK + ks * 4 * BLK + (a & 3) * 32;
                        Wf[n % WIN] = cat8(lds_tr(lp(aM) + o), lds_tr(lp(aM) + o + 256));
                    } else {
                        constexpr int o = ks * 1024;
                        Wf[n % WIN] = cat8(lds_tr(lp(aR) + o), lds_tr(lp(aR) + o + 256));
                    }
                };
                sfor<0, WIN - 1>(rd);
                sfor<0, 162>([&](auto N) {
                    constexpr int n = N.v, kx = n / 9, a = n % 9;
                    if constexpr (n + WIN - 1 < 162) rd(IC<n + WIN - 1>{});
                    const f32x4 z4 = {0.f, 0.f, 0.f, 0.f};
                    if constexpr (first) {
                        acc[a][0] = mfma32(X[kx][0], Wf[n % WIN], kx == 0 ? z4 : acc[a][0]);
                        acc[a][1] = mfma32(X[kx][1], Wf[n % WIN], kx == 0 ? z4 : acc[a][1]);
                    } else {
                        acc[a][0] = mfma32(Wf[n % WIN], X[kx][0], kx == 0 ? z4 : acc[a][0]);
                        acc[a][1] = mfma32(Wf[n % WIN], X[kx][1], kx == 0 ? z4 : acc[a][1]);
                    }
                    SCHED0();
                });
            };
            pass(IC<0>{});
            STAMP(1);
            {
                const int lane = fresh_lane(), q = lane >> 4, c = lane & 15;
                float sum[8];
#pragma unroll
                for (int r = 0; r < 8; ++r) sum[r] = 0.f;
                u32x4 zz[9];
#pragma unroll
                for (int a = 0; a < 9; ++a) {
#pragma unroll
                    for (int b = 0; b < 2; ++b)
#pragma unroll
                        for (int hh = 0; hh < 2; ++hh) {
                            const unsigned zp = gelu_pk(pk_bf16(acc[a][b][2 * hh], acc[a][b][2 * hh + 1]));
                            zz[a][2 * b + hh] = zp;
                            sum[4 * b + 2 * hh] += bf_lo(zp);
                            sum[4 * b + 2 * hh + 1] += bf_hi(zp);
                        }
                    *(LAS u32x4*)stash_at(lds, w, lane, a) = zz[a];
                    asm volatile("" : "+v"(zz[a]));
                    SCHED0();
                }
                float m2[8];
#pragma unroll
                for (int r = 0; r < 8; ++r) { sum[r] = row16_sum(sum[r]) * (1.0f / 144.0f); m2[r] = 0.f; }
#pragma unroll
                for (int a = 0; a < 9; ++a)
#pragma unroll
                    for (int k = 0; k < 4; ++k) {
                        const float d0 = bf_lo(zz[a][k]) - sum[2 * k], d1 = bf_hi(zz[a][k]) - sum[2 * k + 1];
                        m2[2 * k] += d0 * d0; m2[2 * k + 1] += d1 * d1;
                    }
                float mm = sum[0], qv = row16_sum(m2[0]);
#pragma unroll
                for (int r = 1; r < 8; ++r) { const float t = row16_sum(m2[r]); mm = (c == r) ? sum[r] : mm; qv = (c == r) ? t : qv; }
                const u32x4 gr = {__float_as_uint(mm), tag, __float_as_uint(qv), tag};
                const unsigned vo = c < 8 ? (unsigned)(32 * w + 8 * q + c) * 16u : 0x80000000u;
                st16_sc1(swc, vo, WS_STATS + (pi * 16 + pj) * 2048, gr);
                waitl0();
            }
            STAMP(2);
            pass(IC<1>{});
        }
        STAMP(3);
        const int lane = fresh_lane(), c = lane & 15;
#pragma unroll
        for (int a = 0; a < 9; ++a) {
            const u32x4 hp = {pk_bf16(acc[a][0][0], acc[a][0][1]), pk_bf16(acc[a][0][2], acc[a][0][3]), pk_bf16(acc[a][1][0], acc[a][1][1]),
                              pk_bf16(acc[a][1][2], acc[a][1][3])};
            *(LAS u32x4*)stash_at(lds, w, lane, a) = hp;
        }
        waitl0();
        {
            const LAS char* const pA2 = lds + AP + lane * 16;
            const LAS char* const pB = lp(addrB(lds0, w, lane));
            s16x8 A0[9], A1[9], B0[2], B1[2];
#pragma unroll
            for (int a = 0; a < 9; ++a) { acc[a][0] = (f32x4){0.f, 0.f, 0.f, 0.f}; acc[a][1] = (f32x4){0.f, 0.f, 0.f, 0.f}; }
            auto rd = [&](s16x8(&A)[9], s16x8(&B)[2], auto SO, auto KS) {
                constexpr int so = SO.v * SLOT, ks = KS.v;
                B[0] = lds_b128(pB + so + ks * 64);
                B[1] = lds_b128(pB + so + ks * 64 + 512);
#pragma unroll
                for (int a = 0; a < 9; ++a) A[a] = lds_b128(pA2 + so + (ks * 9 + a) * 1024);
            };
            auto mm = [&](s16x8(&A)[9], s16x8(&B)[2]) {
#pragma unroll
                for (int a = 0; a < 9; ++a) { acc[a][0] = mfma32(A[a], B[0], acc[a][0]); acc[a][1] = mfma32(A[a], B[1], acc[a][1]); }
#pragma unroll
                for (int k = 0; k < 11; ++k) { __builtin_amdgcn_sched_group_barrier(0x8, 1, 0); __builtin_amdgcn_sched_group_barrier(0x100, 1, 0); }
                __builtin_amdgcn_sched_group_barrier(0x8, 7, 0);
                SCHED0();
            };
            SCHED0(); bar(); SCHED0();
            STAMP(4);
            rd(A0, B0, IC<2>{}, IC<0>{});
            SCHED0();
            for (int it = 0; it < 8; ++it) {
                sfor<0, 4>([&](auto U) {
                    constexpr int sl = (2 + U.v) & 3, sn = (3 + U.v) & 3;
                    rd(A1, B1, IC<sl>{}, IC<1>{});
                    mm(A0, B0);
                    bar(); SCHED0();
                    rd(A0, B0, IC<sn>{}, IC<0>{});
                    mm(A1, B1);
                });
            }
        }
        STAMP(5);
        {
            float bias[2];
#pragma unroll
            for (int b = 0; b < 2; ++b) bias[b] = __uint_as_float((unsigned)b_sp[128 * pi + 32 * w + 8 * (c >> 2) + 4 * b + (c & 3)] << 16);
            unsigned up[9][2][2];
#pragma unroll
            for (int a = 0; a < 9; ++a) {
                const u32x4 z1 = *(const LAS u32x4*)(a < 8 ? lds + 2 * SLOT + w * 8192 + a * 1024 + lane * 16 : lds + MISC + w * 1024 + lane * 16);
#pragma unroll
                for (int b = 0; b < 2; ++b)
#pragma unroll
                    for (int hh = 0; hh < 2; ++hh) {
                        const unsigned gp = pk_bf16(acc[a][b][2 * hh], acc[a][b][2 * hh + 1]);
                        const unsigned gb = pk_bf16(bf_lo(gp) + bias[b], bf_hi(gp) + bias[b]);
                        const unsigned zp = z1[2 * b + hh];
                        up[a][b][hh] = pk_bf16(bf_lo(zp) * bf_lo(gb), bf_hi(zp) * bf_hi(gb));
                    }
            }
            const unsigned ub = WS_U + ((pi * 2 + (w >> 1)) * 16 + pj) * 18432;
#pragma unroll
            for (int b = 0; b < 2; ++b) {
                const int r = 2 * (w & 1) + b;
#pragma unroll
                for (int p = 0; p < 4; ++p)
                    st16_sc1(swc, lane * 16, ub + (p * 4 + r) * 1024, (u32x4){up[2 * p][b][0], up[2 * p][b][1], up[2 * p + 1][b][0], up[2 * p + 1][b][1]});
                st8_sc1(swc, lane * 8, ub + 16384 + r * 512, (u32x2){up[8][b][0], up[8][b][1]});
            }
            waitv<0>();
            st4_sc1(swc, lane == 0 ? 0u : 0x80000000u, WS_UFLAG + (((pi * 2 + (w >> 1)) * 16 + pj) * 2 + (w & 1)) * 4, tag);
        }
        STAMP(6);
    } else {
        const int pw = wid - 4;
        const int lane = fresh_lane(), q = lane >> 4, c = lane & 15;
        const bool big = pw < 2;
        const i32x4 sx = make_srd(x, S * D * 2), swi = make_srd(w_in, D * F * 2), ssp = make_srd(w_sp, S * S * 2), swo = make_srd(w_out, FH * D * 2);
        const int pos = lane >> 3, ch = lane & 7;
        const unsigned rowB = 128 * pi + 32 * pw + 8 * (((pos >> 1) & 1) + 2 * (pos & 1)) + 4 * (pos >> 2);
        const unsigned vX = rowB * (D * 2) + ch * 16, vSP = rowB * (S * 2) + ch * 16;
        const unsigned vWm = (8 * ((pos & 1) + 2 * (pos >> 2)) + 4 * ((pos >> 1) & 1) + pw) * (F * 2) + ch * 16 + colmap0(pj) * 2;
        const int rho = lane >> 1;
        const unsigned vWr = (32 * pw + 8 * (((rho >> 2) & 1) + 2 * (rho >> 4)) + 4 * ((rho >> 3) & 1) + (rho & 3)) * (F * 2) + (lane & 1) * 16 + colmap8(pj) * 2;
        auto ld_x = [&](int kt, int slot) {
#pragma unroll
            for (int e = 0; e < 4; ++e) dma16(sx, vX, kt * 128 + e * (D * 2), lds0 + slot * SLOT + (4 * pw + e) * BLK);
        };
        auto ld_w = [&](int kt, int half, int slot) {
            const unsigned so = kt * 64 * (F * 2) + half * (FH * 2);
#pragma unroll
            for (int e = 0; e < 4; ++e)
                dma16(swi, vWm, so + (e & 1) * 32 * (F * 2) + (e >> 1) * 128, lds0 + slot * SLOT + AP + ((e >> 1) * 8 + (e & 1) * 4 + pw) * BLK);
            if (big) dma16(swi, vWr, so, lds0 + slot * SLOT + AREM + pw * 1024);
        };
        auto ld_sp = [&](int kt, int slot) {
#pragma unroll
            for (int e = 0; e < 4; ++e) dma16(ssp, vSP, kt * 128 + e * (S * 2), lds0 + slot * SLOT + (4 * pw + e) * BLK);
        };
        auto ld_z = [&](int kt, int slot) {
            const unsigned so = WS_Z2T + (pj * 32 + kt) * 18432;
            dma16x4_sc1(swc, lane * 16, so + pw * 4096, lds0 + slot * SLOT + AP + pw * 4096);
            if (big) dma16_sc1(swc, lane * 16, so + 16384 + pw * 1024, lds0 + slot * SLOT + AP + 16384 + pw * 1024);
        };
#pragma unroll
        for (int t = 0; t < 4; ++t) { ld_x(t, t); ld_w(t, 1, t); }
        waitv2<27, 24>(big); bar();
        waitv2<18, 16>(big); bar();
        waitv2<9, 8>(big); bar();
        for (int t = 2; t < 7; ++t) { ld_x(t + 2, (t + 2) & 3); ld_w(t + 2, 1, (t + 2) & 3); waitv2<9, 8>(big); bar(); }
        ld_w(0, 0, 1); waitv2<5, 4>(big); bar();
        ld_w(1, 0, 2); waitv2<5, 4>(big); bar();
        STAMP(1);
        u32x4 zz[9];
        const int g3 = pj & 7;
        {
            ld_w(2, 0, 3);
#pragma unroll
            for (int n = 0; n < 9; ++n) zz[n] = *(const LAS u32x4*)stash_at(lds, pw, lane, n);
            const unsigned f0 = colmap0(pi) + 32 * pw, f8 = colmap8(pi);
            const unsigned vm = pos * (D * 2) + ch * 16 + g3 * 128;
#pragma unroll
            for (int e = 0; e < 6; ++e)
                dma16(swo, vm, (e < 4 ? f0 + 8 * e : f8 + 8 * (e - 4)) * (D * 2), lds0 + (2 + (e >> 2)) * SLOT + (4 * pw + (e & 3)) * BLK);
            const unsigned rr = lane < 32 ? f0 + lane : f8 + (lane & 15);
            dma16(swo, rr * (D * 2) + 1024 + g3 * 16, 0, lds0 + 3 * SLOT + (4 * pw + 2) * BLK);
            waitv2<12, 11>(big); bar();
        }
        {
            ld_w(3, 0, 0);
            waitv2<5, 4>(big);
            const int qq = (lane >> 2) & 3, pp = lane & 3;
            const LAS char* const m0p = lds + 2 * SLOT + 4 * pw * BLK;
            const LAS char* const m8p = lds + 3 * SLOT + 4 * pw * BLK;
            const LAS char* const rmp = lds + 3 * SLOT + (4 * pw + 2) * BLK;
            const int r4 = 4 * q + qq;
            const unsigned wb = WS_WO + (pj * 16 + pi) * 20736;
            s16x8 fr[5]; s16x4 h0, h1;
#pragma unroll
            for (int n = 0; n < 4; ++n) {
                const int co = (32 * (n >> 1) + 8 * pp + 4 * (n & 1)) * 2;
                fr[n] = cat8(lds_tr(m0p + (r4 >> 3) * BLK + (r4 & 7) * 128 + co), lds_tr(m0p + (2 + (r4 >> 3)) * BLK + (r4 & 7) * 128 + co));
            }
            fr[4] = cat8(lds_tr(rmp + r4 * 16 + (pp & 1) * 8), lds_tr(rmp + (16 + r4) * 16 + (pp & 1) * 8));
            h0 = lds_tr(m8p + (r4 >> 3) * BLK + (r4 & 7) * 128 + (32 * (pw >> 1) + 8 * pp + 4 * (pw & 1)) * 2);
            h1 = lds_tr(rmp + (32 + r4) * 16 + (pp & 1) * 8);
#pragma unroll
            for (int n = 0; n < 4; ++n) st16_sc1(swc, lane * 16, wb + pw * 4608 + n * 1024, __builtin_bit_cast(u32x4, fr[n]));
            const unsigned half16 = c < 8 ? (unsigned)(q * 8 + c) * 16u : 0x80000000u, half8 = c < 8 ? (unsigned)(q * 8 + c) * 8u : 0x80000000u;
            st16_sc1(swc, half16, wb + pw * 4608 + 4096, __builtin_bit_cast(u32x4, fr[4]));
            st8_sc1(swc, lane * 8, wb + 18432 + pw * 512, __builtin_bit_cast(u32x2, h0));
            st8_sc1(swc, pw == 0 ? half8 : 0x80000000u, wb + 18432 + 2048, __builtin_bit_cast(u32x2, h1));
            bar();
        }
        ld_w(4, 0, 1); waitv2<12, 11>(big); bar();
        float mean_r, rinv_r;
        {
            u32x4 gr[8];
            const unsigned vo = (lane >> 5) * 2048 + (32 * pw + (lane & 31)) * 16, so = WS_STATS + pi * 16 * 2048;
            auto fetch = [&]() {
#pragma unroll
                for (int n = 0; n < 8; ++n) asm volatile("buffer_load_dwordx4 %0, %1, %2, %3 offen sc1" : "=v"(gr[n]) : "v"(vo), "s"(swc), "s"(so + n * 4096) : "memory");
            };
            auto pin = [&]() {
                asm volatile("" : "+v"(gr[0]), "+v"(gr[1]), "+v"(gr[2]), "+v"(gr[3]), "+v"(gr[4]), "+v"(gr[5]), "+v"(gr[6]), "+v"(gr[7]));
                bool ok = true;
#pragma unroll
                for (int n = 0; n < 8; ++n) ok = ok && gr[n][1] == tag && gr[n][3] == tag;
                return __all(ok);
            };
            fetch();
            ld_w(5, 0, 2);
            waitv2<5, 4>(big);
            if (!pin()) {
                const unsigned long long t0 = __builtin_amdgcn_s_memrealtime();
                for (;;) {
                    __builtin_amdgcn_s_sleep(1);
                    fetch(); waitv<0>();
                    if (pin() || __builtin_amdgcn_s_memrealtime() - t0 > SPIN_TICKS) break;
                }
            }
            bar();
            ld_w(6, 0, 3);
            float ms = 0.f;
#pragma unroll
            for (int n = 0; n < 8; ++n) ms += __uint_as_float(gr[n][0]);
            ms += __shfl_xor(ms, 32);
            const float mean = ms * (1.0f / 16.0f);
            float qs = 0.f;
#pragma unroll
            for (int n = 0; n < 8; ++n) { const float d = __uint_as_float(gr[n][0]) - mean; qs += __uint_as_float(gr[n][2]) + 144.0f * d * d; }
            qs += __shfl_xor(qs, 32);
            mean_r = rbf(mean);
            rinv_r = 1.0f / rbf(__builtin_sqrtf(rbf(rbf(qs * (1.0f / 2304.0f)) + 1e-5f)));
        }
        {
            LAS char* const tb = lds + MISC + 4096 + pw * 256;
            if (lane < 32) *(LAS f32x2*)(tb + lane * 8) = (f32x2){mean_r, rinv_r};
            waitl0();
            f32x4 st[4];
#pragma unroll
            for (int k = 0; k < 4; ++k) st[k] = *(const LAS f32x4*)(tb + q * 64 + k * 16);
            const unsigned zb = WS_Z2T + (pj * 64 + pi * 4 + pw) * 9216;
            auto norm = [&](int a) {
                u32x4 o;
#pragma unroll
                for (int k = 0; k < 4; ++k) {
                    const unsigned dp = pk_bf16(bf_lo(zz[a][k]) - st[k][0], bf_hi(zz[a][k]) - st[k][2]);
                    o[k] = pk_bf16(bf_lo(dp) * st[k][1], bf_hi(dp) * st[k][3]);
                }
                st16_sc1(swc, lane * 16, zb + a * 1024, o);
            };
#pragma unroll
            for (int a = 0; a < 5; ++a) norm(a);
            waitv2<10, 9>(big); bar();
            ld_w(7, 0, 0);
#pragma unroll
            for (int a = 5; a < 9; ++a) norm(a);
            waitv2<14, 13>(big); bar();
        }
        ld_w(8, 0, 1);
        waitv2<5, 4>(big);
        st4_sc1(swc, lane == 0 ? 0u : 0x80000000u, WS_ZFLAG + (pj * 64 + pi * 4 + pw) * 4, tag);
        STAMP(2);
        bar();
        ld_sp(0, 2); waitv<5>(); bar();
        ld_sp(1, 3);
        {
            const unsigned long long t0 = __builtin_amdgcn_s_memrealtime();
            for (;;) {
                unsigned f;
                asm volatile("buffer_load_dword %0, %1, %2, %3 offen sc1\n\ts_waitcnt vmcnt(0)" : "=v"(f) : "v"(lane * 4), "s"(swc), "s"(WS_ZFLAG + pj * 256) : "memory");
                if (__all(f == tag) || __builtin_amdgcn_s_memrealtime() - t0 > SPIN_TICKS) break;
                __builtin_amdgcn_s_sleep(1);
            }
        }
        STAMP(3);
        ld_z(0, 2); ld_z(1, 3);
        waitv2<5, 4>(big); bar();
#pragma unroll
        for (int n = 0; n < 9; ++n) zz[n] = *(const LAS u32x4*)stash_at(lds, pw, lane, n);
        waitl0();
        asm volatile("" : "+v"(zz[0]), "+v"(zz[1]), "+v"(zz[2]), "+v"(zz[3]), "+v"(zz[4]), "+v"(zz[5]), "+v"(zz[6]), "+v"(zz[7]), "+v"(zz[8]));
        sfor<0, 9>([&](auto N) {
            constexpr int n = N.v;
            ld_sp(n + 2, n & 3); ld_z(n + 2, n & 3);
#pragma unroll
            for (int k = 0; k < 4; ++k) zz[n][k] = gelu_pk(zz[n][k]);
            asm volatile("" : "+v"(zz[n]));
            waitv2<9, 8>(big); bar();
        });
        for (int t = 9; t < 30; ++t) { ld_sp(t + 2, t & 3); ld_z(t + 2, t & 3); waitv2<9, 8>(big); bar(); }
#pragma unroll
        for (int n = 0; n < 9; ++n) *(LAS u32x4*)(n < 8 ? lds + 2 * SLOT + pw * 8192 + n * 1024 + lane * 16 : lds + MISC + pw * 1024 + lane * 16) = zz[n];
        waitl0(); waitv<0>(); bar();
        bar();
        STAMP(5);
    }

    {
        const int v = wid, h3 = pj >> 3, g3 = pj & 7;
        const int lane = fresh_lane(), q = lane >> 4, c = lane & 15;
        const __amdgpu_buffer_rsrc_t rw = __builtin_amdgcn_make_buffer_rsrc((void*)ws, 0, WS_END, 0x00020000);
        const unsigned wb = WS_WO + (pj * 16 + 2 * v) * 20736, ub = WS_U + ((pi * 2 + h3) * 16 + 2 * v) * 18432;
        const unsigned l16 = lane * 16, l8 = lane * 8, h16 = (q * 8 + (c & 7)) * 16, h8 = (q * 8 + (c & 7)) * 8;
        constexpr int WW = 5, UW = 3;
        u32x4 W[WW][5], U[UW][4];
        auto ldW = [&](auto SS) {
            constexpr int s = SS.v, pn = s / 5, p = s % 5;
            if constexpr (p < 4) {
#pragma unroll
                for (int n = 0; n < 4; ++n) W[s % WW][n] = __builtin_amdgcn_raw_buffer_load_b128(rw, l16, wb + pn * 20736 + p * 4608 + n * 1024, 16);
                W[s % WW][4] = __builtin_amdgcn_raw_buffer_load_b128(rw, h16, wb + pn * 20736 + p * 4608 + 4096, 16);
            } else {
#pragma unroll
                for (int n = 0; n < 5; ++n) {
                    const u32x2 t = __builtin_amdgcn_raw_buffer_load_b64(rw, n < 4 ? l8 : h8, wb + pn * 20736 + 18432 + n * 512, 16);
                    W[s % WW][n][0] = t[0]; W[s % WW][n][1] = t[1];
                }
            }
        };
        auto ldU = [&](auto SS) {
            constexpr int s = SS.v, pn = s / 5, p = s % 5;
#pragma unroll
            for (int r = 0; r < 4; ++r) {
                if constexpr (p < 4) U[s % UW][r] = __builtin_amdgcn_raw_buffer_load_b128(rw, l16, ub + pn * 18432 + (p * 4 + r) * 1024, 16);
                else {
                    const u32x2 t = __builtin_amdgcn_raw_buffer_load_b64(rw, l8, ub + pn * 18432 + 16384 + r * 512, 16);
                    U[s % UW][r][0] = t[0]; U[s % UW][r][1] = t[1];
                }
            }
        };
        sfor<0, WW>(ldW);
        SCHED0();
        {
            const unsigned long long t0 = __builtin_amdgcn_s_memrealtime();
            for (;;) {
                unsigned f;
                asm volatile("buffer_load_dword %0, %1, %2, %3 offen sc1\n\ts_waitcnt vmcnt(0)" : "=v"(f) : "v"((lane & 3) * 4), "s"(swc), "s"(WS_UFLAG + ((pi * 2 + h3) * 16 + 2 * v) * 8) : "memory");
                if (__all(f == tag) || __builtin_amdgcn_s_memrealtime() - t0 > SPIN_TICKS) break;
                __builtin_amdgcn_s_sleep(1);
            }
        }
        STAMP(7);
        SCHED0();
        sfor<0, UW>(ldU);
        SCHED0();
        f32x4 o[5][4];
        sfor<0, 10>([&](auto SS) {
            constexpr int s = SS.v, p = s % 5;
            const f32x4 z4 = {0.f, 0.f, 0.f, 0.f};
#pragma unroll
            for (int r = 0; r < 4; ++r)
#pragma unroll
                for (int n = 0; n < 5; ++n) {
                    if constexpr (p < 4) o[n][r] = mfma32(__builtin_bit_cast(s16x8, W[s % WW][n]), __builtin_bit_cast(s16x8, U[s % UW][r]), s == 0 ? z4 : o[n][r]);
                    else {
                        const u32x2 a2 = {W[s % WW][n][0], W[s % WW][n][1]}, b2 = {U[s % UW][r][0], U[s % UW][r][1]};
                        o[n][r] = __builtin_amdgcn_mfma_f32_16x16x16bf16_1k(__builtin_bit_cast(s16x4, a2), __builtin_bit_cast(s16x4, b2), o[n][r], 0, 0, 0);
                    }
                }
            SCHED0();
            if constexpr (s + UW < 10) ldU(IC<s + UW>{});
            if constexpr (s + WW < 10) ldW(IC<s + WW>{});
            SCHED0();
        });
        STAMP(8);
        bar();
#pragma unroll
        for (int r = 0; r < 4; ++r) {
#pragma unroll
            for (int n = 0; n < 4; ++n)
                if (2 * r + (n >> 1) != v) *(LAS f32x4*)(lds + ((r * 4 + n) * 8 + v) * 1024 + lane * 16) = o[n][r];
            if (2 * r != v && lane < 32) *(LAS f32x4*)(lds + 131072 + (r * 8 + v) * 512 + lane * 16) = o[4][r];
        }
        waitl0(); bar();
        {
            const int r = v >> 1, m = v & 1;
            f32x4 own0 = o[0][0], own1 = o[1][0], ownh = o[4][0];
#pragma unroll
            for (int k = 1; k < 8; ++k)
                if (v == k) { own0 = o[2 * (k & 1)][k >> 1]; own1 = o[2 * (k & 1) + 1][k >> 1]; ownh = o[4][k >> 1]; }
            f32x4 s0 = {0.f, 0.f, 0.f, 0.f}, s1 = s0, sh = s0;
#pragma unroll
            for (int k = 0; k < 8; ++k) {
                const f32x4 t0 = *(const LAS f32x4*)(lds + ((r * 4 + 2 * m) * 8 + k) * 1024 + lane * 16);
                const f32x4 t1 = *(const LAS f32x4*)(lds + ((r * 4 + 2 * m + 1) * 8 + k) * 1024 + lane * 16);
                const f32x4 th = *(const LAS f32x4*)(lds + 131072 + (r * 8 + k) * 512 + (lane & 31) * 16);
                s0 += (k == v) ? own0 : t0; s1 += (k == v) ? own1 : t1; sh += (k == v) ? ownh : th;
            }
            const int t = 128 * pi + 64 * h3 + 32 * (r >> 1) + 8 * (c >> 2) + 4 * (r & 1) + (c & 3);
            u16* orow = out + (size_t)t * D;
            *(u32x4*)(orow + 64 * g3 + 32 * m + 8 * q) = (u32x4){pk_bf16(s0[0], s0[1]), pk_bf16(s0[2], s0[3]), pk_bf16(s1[0], s1[1]), pk_bf16(s1[2], s1[3])};
            if (m == 0 && lane < 32) *(u32x2*)(orow + 512 + 8 * g3 + 4 * q) = (u32x2){pk_bf16(sh[0], sh[1]), pk_bf16(sh[2], sh[3])};
        }
        if (blockIdx.x == 0 && v == 0 && lane == 0) *(unsigned*)(ws + WS_EPOCH) = tag;
        STAMP(9);
    }
}

extern "C" void kernel_launch(void* const* d_in, const int* in_sizes, int n_in, void* d_out, int out_size, void* d_ws, size_t ws_size,
                              hipStream_t stream) {
    (void)in_sizes; (void)n_in; (void)out_size; (void)ws_size;
    (void)hipFuncSetAttribute((const void*)gmlp_kernel, hipFuncAttributeMaxDynamicSharedMemorySize, LDS_BYTES);
    gmlp_kernel<<<256, 512, LDS_BYTES, stream>>>((const u16*)d_in[0], (const u16*)d_in[1], (const u16*)d_in[2], (const u16*)d_in[3],
                                                 (const u16*)d_in[4], (u16*)d_out, (char*)d_ws);
}
```

```cpp
#include <hip/hip_runtime.h>
#include <hip/hip_bf16.h>
#include <stdint.h>
#include <utility>

typedef unsigned short u16;
typedef short s16x4 __attribute__((ext_vector_type(4)));
typedef short s16x8 __attribute__((ext_vector_type(8)));
typedef __bf16 bf16x2 __attribute__((ext_vector_type(2)));
typedef __bf16 bf16x8 __attribute__((ext_vector_type(8)));
typedef float f32x2 __attribute__((ext_vector_type(2)));
typedef float f32x4 __attribute__((ext_vector_type(4)));
typedef unsigned u32x2 __attribute__((ext_vector_type(2)));
typedef unsigned u32x4 __attribute__((ext_vector_type(4)));
typedef int i32x4 __attribute__((ext_vector_type(4)));
#define LAS __attribute__((address_space(3)))
#define DEVI __device__ __forceinline__

constexpr int S = 2048, D = 576, F = 4608, FH = 2304;
constexpr int BLK = 1056;
constexpr int AP = 16 * BLK;
constexpr int AREM = AP + 16 * BLK;
constexpr int SLOT = AREM + 2048;
constexpr int MISC = 4 * SLOT;
constexpr int RED = 36864;
constexpr int RDY = MISC + 16384;
constexpr int DONE = RDY + 256;
constexpr int HOFF = RDY + 512;
constexpr int HOF2 = RDY + 1024;
constexpr int STAB = MISC + 18432;
constexpr int STASH8 = MISC + 4096;
constexpr int REMX = MISC + 8192;
constexpr int LDS_BYTES = 163840;
constexpr unsigned WS_EPOCH = 0;
constexpr unsigned WS_ZFLAG = 4096;
constexpr unsigned WS_UFLAG = 8192;
constexpr unsigned WS_WFLAG = 12288;
constexpr unsigned WS_STATS = 16384;
constexpr unsigned WS_Z2T = 2u << 20;
constexpr unsigned WS_U = WS_Z2T + 16u * 64 * 9216;
constexpr unsigned WS_WO = WS_U + 512u * 18432;
constexpr unsigned WS_END = WS_WO + 256u * 20736;
constexpr unsigned WS_DBG = 32u << 20;

#define SLEEPN 1
#define ZLD "sc1"
#define STB "sc1"
template <int N> struct IC { static constexpr int v = N; };
template <int B, class Fn, int... I> DEVI void sfor_(Fn& f, std::integer_sequence<int, I...>) { (f(IC<B + I>{}), ...); }
template <int B, int E, class Fn> DEVI void sfor(Fn&& f) { sfor_<B>(f, std::make_integer_sequence<int, E - B>{}); }

DEVI i32x4 make_srd(const void* p, unsigned bytes) {
    i32x4 r; const unsigned long long a = (unsigned long long)p;
    r.x = (int)(unsigned)a; r.y = (int)((unsigned)(a >> 32) & 0xffffu); r.z = (int)bytes; r.w = 0x00020000;
    return r;
}
DEVI void dma16(i32x4 srd, unsigned voff, unsigned soff, unsigned lds) {
    asm volatile("s_mov_b32 m0, %3\n\ts_nop 0\n\tbuffer_load_dwordx4 %0, %1, %2 offen lds" ::"v"(voff), "s"(srd), "s"(soff), "s"(lds) : "memory");
}
DEVI void dma16_sc1(i32x4 srd, unsigned voff, unsigned soff, unsigned lds) {
    asm volatile("s_mov_b32 m0, %3\n\ts_nop 0\n\tbuffer_load_dwordx4 %0, %1, %2 offen " ZLD " lds" ::"v"(voff), "s"(srd), "s"(soff), "s"(lds) : "memory");
}
DEVI void dma16x4_sc1(i32x4 srd, unsigned voff, unsigned soff, unsigned lds) {
    asm volatile("s_mov_b32 m0, %3\n\ts_nop 0\n\tbuffer_load_dwordx4 %0, %1, %2 offen " ZLD " lds\n\t"
                 "buffer_load_dwordx4 %0, %1, %2 offen offset:1024 " ZLD " lds\n\t"
                 "buffer_load_dwordx4 %0, %1, %2 offen offset:2048 " ZLD " lds\n\t"
                 "buffer_load_dwordx4 %0, %1, %2 offen offset:3072 " ZLD " lds" ::"v"(voff), "s"(srd), "s"(soff), "s"(lds) : "memory");
}
struct V4 { unsigned v[4]; };
DEVI V4 strided4(unsigned base, unsigned stride) { return V4{{base, base + stride - BLK, base + 2 * (stride - BLK), base + 3 * (stride - BLK)}}; }
DEVI void dma16x4b(i32x4 srd, const V4& v, unsigned soff, unsigned lds) {
    asm volatile("s_mov_b32 m0, %6\n\ts_nop 0\n\tbuffer_load_dwordx4 %0, %4, %5 offen lds\n\t"
                 "buffer_load_dwordx4 %1, %4, %5 offen offset:1056 lds\n\t"
                 "buffer_load_dwordx4 %2, %4, %5 offen offset:2112 lds\n\t"
                 "buffer_load_dwordx4 %3, %4, %5 offen offset:3168 lds"
                 ::"v"(v.v[0]), "v"(v.v[1]), "v"(v.v[2]), "v"(v.v[3]), "s"(srd), "s"(soff), "s"(lds) : "memory");
}
template <int LS, int MS> DEVI void dma16x4s(i32x4 srd, unsigned voff, unsigned soff, unsigned lds) {
    unsigned t;
    asm volatile("s_mov_b32 m0, %4\n\ts_add_u32 %0, %3, %6\n\tbuffer_load_dwordx4 %1, %2, %3 offen lds\n\t"
                 "s_add_u32 m0, m0, %5\n\ts_nop 0\n\tbuffer_load_dwordx4 %1, %2, %0 offen lds\n\t"
                 "s_add_u32 m0, m0, %5\n\ts_add_u32 %0, %0, %6\n\tbuffer_load_dwordx4 %1, %2, %0 offen lds\n\t"
                 "s_add_u32 m0, m0, %5\n\ts_add_u32 %0, %0, %6\n\tbuffer_load_dwordx4 %1, %2, %0 offen lds"
                 : "=&s"(t) : "v"(voff), "s"(srd), "s"(soff), "s"(lds), "i"(LS), "i"(MS) : "memory", "scc");
}
DEVI void dma16_half(i32x4 srd, unsigned voff, unsigned soff, unsigned lds) {
    asm volatile("s_mov_b32 m0, %3\n\ts_mov_b64 exec, 0xffffffff\n\tbuffer_load_dwordx4 %0, %1, %2 offen lds\n\ts_mov_b64 exec, -1" ::"v"(voff), "s"(srd), "s"(soff), "s"(lds) : "memory");
}
DEVI void dma16_half_sc1(i32x4 srd, unsigned voff, unsigned soff, unsigned lds) {
    asm volatile("s_mov_b32 m0, %3\n\ts_mov_b64 exec, 0xffffffff\n\tbuffer_load_dwordx4 %0, %1, %2 offen " ZLD " lds\n\ts_mov_b64 exec, -1" ::"v"(voff), "s"(srd), "s"(soff), "s"(lds) : "memory");
}
DEVI void mfma_acc(f32x4& c, s16x8 a, s16x8 b) { asm volatile("v_mfma_f32_16x16x32_bf16 %0, %1, %2, %0" : "+v"(c) : "v"(a), "v"(b)); }
DEVI void mfma_new(f32x4& c, s16x8 a, s16x8 b) { asm volatile("v_mfma_f32_16x16x32_bf16 %0, %1, %2, 0" : "=v"(c) : "v"(a), "v"(b)); }
DEVI void st16_sc1(i32x4 srd, unsigned voff, unsigned soff, u32x4 v) {
    asm volatile("buffer_store_dwordx4 %0, %1, %2, %3 offen " STB "\n\ts_nop 1" ::"v"(v), "v"(voff), "s"(srd), "s"(soff) : "memory");
}
DEVI void st8_sc1(i32x4 srd, unsigned voff, unsigned soff, u32x2 v) {
    asm volatile("buffer_store_dwordx2 %0, %1, %2, %3 offen " STB "\n\ts_nop 1" ::"v"(v), "v"(voff), "s"(srd), "s"(soff) : "memory");
}
DEVI void st4_sc1(i32x4 srd, unsigned voff, unsigned soff, unsigned v) {
    asm volatile("buffer_store_dword %0, %1, %2, %3 offen sc1\n\ts_nop 1" ::"v"(v), "v"(voff), "s"(srd), "s"(soff) : "memory");
}
template <int N> DEVI void waitv() { asm volatile("s_waitcnt vmcnt(%0)" ::"i"(N) : "memory"); }
DEVI void waitl0() { asm volatile("s_waitcnt lgkmcnt(0)" ::: "memory"); }
DEVI void bar() { asm volatile("s_barrier" ::: "memory"); }
#define SCHED0() __builtin_amdgcn_sched_barrier(0)

DEVI unsigned pk_bf16(float lo, float hi) { f32x2 x = {lo, hi}; return __builtin_bit_cast(unsigned, __builtin_convertvector(x, bf16x2)); }
DEVI float bf_lo(unsigned p) { return __uint_as_float(p << 16); }
DEVI float bf_hi(unsigned p) { return __uint_as_float(p & 0xffff0000u); }
DEVI float rbf(float x) { return bf_lo(pk_bf16(x, x)); }
DEVI f32x2 gelu2(f32x2 h) {
    const float c1 = -2.302208198f, c2 = -0.1029432397f;
    f32x2 t = (h * h) * c2 + c1;
    t = t * h;
    f32x2 e = {__builtin_amdgcn_exp2f(t.x), __builtin_amdgcn_exp2f(t.y)};
    e = e + 1.0f;
    f32x2 r = {__builtin_amdgcn_rcpf(e.x), __builtin_amdgcn_rcpf(e.y)};
    return h * r;
}
DEVI float solo(float x) { asm("" : "+v"(x)); return x; }
DEVI f32x4 add4(f32x4 a, f32x4 b) { return (f32x4){solo(a[0] + b[0]), solo(a[1] + b[1]), solo(a[2] + b[2]), solo(a[3] + b[3])}; }
DEVI float gelu1(float h) {
    const float t = __builtin_fmaf(h * h, -0.1029432397f, -2.302208198f) * h;
    return solo(h * __builtin_amdgcn_rcpf(__builtin_amdgcn_exp2f(t) + 1.0f));
}
DEVI unsigned gelu_pk(unsigned hp) { return pk_bf16(gelu1(bf_lo(hp)), gelu1(bf_hi(hp))); }
template <int CTRL> DEVI float dpp_add(float x) {
    return x + __int_as_float(__builtin_amdgcn_update_dpp(0, __float_as_int(x), CTRL, 0xf, 0xf, false));
}
DEVI float row16_sum(float x) { x = dpp_add<0x128>(x); x = dpp_add<0x124>(x); x = dpp_add<0x122>(x); x = dpp_add<0x121>(x); return x; }
DEVI void publish_stats(const f32x2 (&s1)[4], const f32x2 (&s2)[4], i32x4 swc, unsigned soff, int w, int lane, unsigned tag) {
    const int q = lane >> 4, c = lane & 15;
    float a = row16_sum(s1[0].x), b = row16_sum(s2[0].x);
#pragma unroll
    for (int r = 1; r < 8; ++r) {
        const float ta = row16_sum(s1[r >> 1][r & 1]), tb = row16_sum(s2[r >> 1][r & 1]);
        a = (c == r) ? ta : a; b = (c == r) ? tb : b;
    }
    asm volatile("buffer_store_dwordx4 %0, %1, %2, %3 offen sc1\n\ts_nop 1" ::"v"((u32x4){__float_as_uint(a), tag, __float_as_uint(b), tag}), "v"(c < 8 ? (unsigned)(32 * w + 8 * q + c) * 16u : 0x80000000u), "s"(swc), "s"(soff) : "memory");
}
DEVI f32x4 mfma32(s16x8 a, s16x8 b, f32x4 c) {
    return __builtin_amdgcn_mfma_f32_16x16x32_bf16(__builtin_bit_cast(bf16x8, a), __builtin_bit_cast(bf16x8, b), c, 0, 0, 0);
}
DEVI s16x8 lds_b128(const LAS char* p) { return *(const LAS s16x8*)p; }
DEVI s16x4 lds_tr(const LAS char* p) { return __builtin_amdgcn_ds_read_tr16_b64_v4i16((LAS s16x4*)p); }
DEVI s16x8 cat8(s16x4 lo, s16x4 hi) { return __builtin_shufflevector(lo, hi, 0, 1, 2, 3, 4, 5, 6, 7); }
DEVI int colmap8(int j) { return 576 * (j >> 2) + 512 + 16 * (j & 3); }
DEVI int colmap0(int j) { return 576 * (j >> 2) + 128 * (j & 3); }
DEVI int fresh_lane() { int l; asm volatile("v_mbcnt_lo_u32_b32 %0, -1, 0\n\tv_mbcnt_hi_u32_b32 %0, -1, %0" : "=v"(l)); return l; }
template <int DLT> DEVI void bump(unsigned& a) { asm volatile("v_add_u32 %0, %1, %0" : "+v"(a) : "i"(DLT)); }
DEVI unsigned opaque(unsigned x) { asm volatile("" : "+v"(x)); return x; }
DEVI unsigned long long lanes_below(unsigned v, unsigned need) { unsigned long long m; asm volatile("v_cmp_gt_u32 %0, %1, %2" : "=s"(m) : "s"(need), "v"(v)); return m; }
DEVI const LAS char* lp(unsigned a) { return (const LAS char*)(size_t)a; }
DEVI unsigned addrB(unsigned lds0, int w, int lane) { const int q = lane >> 4, c = lane & 15; return lds0 + (4 * w + (c & 3)) * BLK + ((c >> 3) + 2 * ((c >> 2) & 1)) * 128 + q * 16; }
constexpr int g1_main(int T) { return (T >= 13 && T <= 16) ? ((T - 11) & 3) * SLOT : (T & 3) * SLOT + AP; }
constexpr int g1_rem(int T) { return (T >= 13 && T <= 16) ? REMX + (T - 13) * 2048 : (T & 3) * SLOT + AREM; }
DEVI int e2b(int e) { return e; }
DEVI LAS char* stash_at(LAS char* lds, int w, int lane, int n) {
    return n < 4 ? lds + (4 * w + n) * BLK + lane * 16 : n < 8 ? lds + SLOT + (4 * w + n - 4) * BLK + lane * 16 : lds + STASH8 + w * 1024 + lane * 16;
}
DEVI void post(LAS char* lds, int at, int w, int lane, unsigned v) { *(volatile LAS unsigned*)(lds + at + w * 64 + (lane & 15) * 4) = v; }
DEVI unsigned peek4(LAS char* lds, int at, int lane) { return *(volatile LAS unsigned*)(lds + at + lane * 4); }
DEVI void spin4(LAS char* lds, int at, int lane, unsigned need) {
#pragma nounroll
    for (int n = 0; n < (1 << 18); ++n) { if (__all(peek4(lds, at, lane) >= need)) break; __builtin_amdgcn_s_sleep(SLEEPN); }
    asm volatile("" ::: "memory");
}
DEVI void spin1(LAS char* lds, int at, int w, unsigned need) {
#pragma nounroll
    for (int n = 0; n < (1 << 18); ++n) { if (__all(*(volatile LAS unsigned*)(lds + at + w * 64) >= need)) break; __builtin_amdgcn_s_sleep(SLEEPN); }
    asm volatile("" ::: "memory");
}
constexpr unsigned long long SPIN_TICKS = 400000ull;

DEVI void poll_flags(i32x4 srd, unsigned voff, unsigned soff, unsigned tag) {
    const unsigned long long t0 = __builtin_amdgcn_s_memrealtime();
    for (;;) {
        unsigned f;
        asm volatile("buffer_load_dword %0, %1, %2, %3 offen sc1\n\ts_waitcnt vmcnt(0)" : "=v"(f) : "v"(voff), "s"(srd), "s"(soff) : "memory");
        if (__all(f == tag) || __builtin_amdgcn_s_memrealtime() - t0 > SPIN_TICKS) break;
        __builtin_amdgcn_s_sleep(1);
    }
}
#define STAMP(k) do {} while (0)
#define CSTAMP(k) do {} while (0)
#define COUNT(k) do {} while (0)
#define VSTAMP(idx) do {} while (0)
#define STAMP_DECL do {} while (0)
#define STAMP_FLUSH() do {} while (0)

#define DMAUNR 4
#define WEARLY 10
#define UWIN 8
#define PACE() do {} while (0)
#define SEENAT 0
#define DONEAT 8
#define G1SEEN 9
#define G2BASE
#define BARAT 17
#define G1WIN 6
#define PRODPRIO 2
#define ROT1 ((jl + 2 * il) % 9)
#define ROT2 (4 * jl)
#define RPM 1
#define WIN2 1
#define PHASE2 0
__global__ __launch_bounds__(512, 2) void gmlp_kernel(const u16* __restrict__ x, const u16* __restrict__ w_in, const u16* __restrict__ w_sp,
                                                      const u16* __restrict__ b_sp, const u16* __restrict__ w_out, u16* __restrict__ out,
                                                      char* __restrict__ ws) {
#if defined(__HIP_DEVICE_COMPILE__)
    extern __shared__ __attribute__((aligned(16))) char shm_[];
    LAS char* const lds = (LAS char*)shm_;
    const unsigned lds0 = (unsigned)(size_t)lds;
    const int wid = __builtin_amdgcn_readfirstlane(threadIdx.x >> 6);
    const int xcd = blockIdx.x & 7, loc = blockIdx.x >> 3;
    const int il = loc & 3, jl = loc >> 2;
    const int pi = 4 * (xcd >> 1) + il, pj = 8 * (xcd & 1) + jl;
    unsigned tag;
    auto get_tag = [&]() {
        asm volatile("s_load_dword %0, %1, 0x0\n\ts_waitcnt lgkmcnt(0)" : "=s"(tag) : "s"(ws) : "memory");
        tag += 1u;
    };
    i32x4 swc = make_srd(ws, WS_END);
    const __amdgpu_buffer_rsrc_t rw = __builtin_amdgcn_make_buffer_rsrc((void*)ws, 0, WS_END, 0x00020000);
    STAMP_DECL;
    STAMP(0);
    if (wid == 0) { const int l = fresh_lane(); *(LAS u32x4*)(lds + RDY + l * 16) = (u32x4){0u, 0u, 0u, 0u}; *(LAS unsigned*)(lds + HOF2 + l * 4) = 0u; waitl0(); }
    u32x4 W[10][5];
    auto load_wo = [&](auto S0, auto S1) {
        const int lane = fresh_lane(), hl = (lane >> 4) * 8 + (lane & 7);
        const unsigned wb = WS_WO + (pj * 16 + 2 * wid) * 20736;
#pragma unroll
        for (int s = S0.v; s < S1.v; ++s)
#pragma unroll
            for (int n = 0; n < 5; ++n) {
                const unsigned pb = wb + (s / 5) * 20736;
                if (s % 5 < 4) W[s][n] = __builtin_amdgcn_raw_buffer_load_b128(rw, (n < 4 ? lane : hl) * 16, n < 4 ? pb + (s % 5) * 4096 + n * 1024 : pb + 18432 + (s % 5) * 512, 16);
                else {
                    const u32x2 t = __builtin_amdgcn_raw_buffer_load_b64(rw, (n < 4 ? lane : hl) * 8, n < 4 ? pb + 16384 + n * 512 : pb + 20480, 16);
                    W[s][n] = (u32x4){t[0], t[1], 0u, 0u};
                }
            }
    };

    if (wid < 4) {
        const int w = wid;
        f32x4 acc[9][2];
        {
            unsigned aB, aM, aR;
            unsigned seen = 0, aRdy, aDone, val;
            unsigned long long lag = 0;
            get_tag();
            {
                const int lane = fresh_lane(), q = lane >> 4, qq = (lane >> 2) & 3, pp = lane & 3;
                aB = addrB(lds0, w, lane);
                aRdy = opaque(lds0 + RDY + lane * 4); aDone = opaque(lds0 + DONE + w * 64 + (lane & 15) * 4);
                aM = lds0 + AP + qq * BLK + ((q & 1) + 4 * (q >> 1)) * 128 + pp * 8;
                aR = lds0 + AREM + (qq + 4 * (q & 1) + 16 * (q >> 1)) * 32 + pp * 8;
            }
            s16x8 X[18][2];
            constexpr int WIN = G1WIN;
            s16x4 Wf[WIN][2];
            auto pass = [&](auto PA) {
                constexpr bool first = PA.v == 0;
                constexpr int T0 = first ? 0 : 9;
                auto rd = [&](auto N, auto H) {
                    constexpr int n = N.v, h = H.v, kt = n / 18, ks = (n % 18) / 9, a = n % 9;
                    constexpr int T = T0 + kt;
                    if constexpr (n % 18 == 0 && h == 0) {
                        if constexpr (first) bar();
                        else if (kt == 0 || __builtin_expect(lag != 0, 0)) { COUNT(14); spin4(lds, RDY, fresh_lane(), T + 1); }
                        SCHED0();
                    }
                    if constexpr (first && a == 0) X[2 * kt + ks][h] = lds_b128(lp(aB) + ks * 64 + h * 512);
                    if constexpr (a < 8) Wf[n % WIN][h] = lds_tr(lp(aM) + (a >> 2) * 8 * BLK + ks * 4 * BLK + (a & 3) * 32 + h * 256);
                    else Wf[n % WIN][h] = lds_tr(lp(aR) + ks * 1024 + h * 256);
                    SCHED0();
                    if constexpr (!first && n % 18 == G1SEEN && h == 0 && kt < 8) seen = *(volatile LAS unsigned*)(size_t)aRdy;
                    if constexpr (!first && n % 18 == 17 && h == 0 && kt < 8) lag = lanes_below(seen, T0 + kt + 2);
                    if constexpr (first && n % 18 == 10 && h == 0 && kt < 8) bump<(((T + 1) & 3) - (T & 3)) * SLOT>(aB);
                    if constexpr (n % 18 == 16 && h == 1 && T < 17) bump<g1_main(T + 1) - g1_main(T)>(aM);
                    if constexpr (n % 18 == 1 && h == 0 && T > 0) bump<g1_rem(T) - g1_rem(T - 1)>(aR);
                };
                sfor<0, WIN - 1>([&](auto N) { rd(N, IC<0>{}); rd(N, IC<1>{}); });
                sfor<0, 324>([&](auto M) {
                    constexpr int n = M.v >> 1, b = M.v & 1, kx = n / 9, a = n % 9;
                    const s16x8 wf = cat8(Wf[n % WIN][0], Wf[n % WIN][1]);
                    if constexpr (first) { if constexpr (kx == 0) mfma_new(acc[a][b], X[kx][b], wf); else mfma_acc(acc[a][b], X[kx][b], wf); }
                    else { if constexpr (kx == 0) mfma_new(acc[a][b], wf, X[kx][b]); else mfma_acc(acc[a][b], wf, X[kx][b]); }
                    SCHED0();
                    if constexpr (n + WIN - 1 < 162) rd(IC<n + WIN - 1>{}, IC<b>{});
                    if constexpr (!first && n % 18 == 16 && b == 1) val = opaque(T0 + n / 18 + 1);
                    if constexpr (!first && n % 18 == 17 && b == 0) *(volatile LAS unsigned*)(size_t)aDone = val;
                    SCHED0();
                });
                asm volatile("s_nop 7" ::: "memory");
                SCHED0();
            };
            CSTAMP(18);
            pass(IC<0>{});
            bar();
            STAMP(1); CSTAMP(19);
            {
                const int lane = fresh_lane();
                f32x2 s1[4] = {}, s2[4] = {};
#pragma unroll
                for (int a = 0; a < 9; ++a) {
                    u32x4 z;
#pragma unroll
                    for (int k = 0; k < 4; ++k) {
                        const f32x2 y = gelu2((f32x2){acc[a][k >> 1][2 * (k & 1)], acc[a][k >> 1][2 * (k & 1) + 1]});
                        s1[k] += y; s2[k] += y * y;
                        z[k] = pk_bf16(y.x, y.y);
                    }
                    *(LAS u32x4*)stash_at(lds, w, lane, a) = z;
                    SCHED0();
                }
                publish_stats(s1, s2, swc, WS_STATS + (pi * 16 + pj) * 2048, w, lane, tag);
                waitl0(); bar();
            }
            STAMP(2);
            CSTAMP(20);
            pass(IC<1>{});
            CSTAMP(21);
        }
        STAMP(3);
        const int lane = fresh_lane(), c = lane & 15;
        u32x4 z1[9];
#pragma unroll
        for (int a = 0; a < 9; ++a) {
#pragma unroll
            for (int k = 0; k < 4; ++k) {
                const f32x2 y = gelu2((f32x2){acc[a][k >> 1][2 * (k & 1)], acc[a][k >> 1][2 * (k & 1) + 1]});
                z1[a][k] = pk_bf16(y.x, y.y);
            }
            asm volatile("" : "+v"(z1[a]));
        }
        float bias[2];
#pragma unroll
        for (int b = 0; b < 2; ++b) bias[b] = __uint_as_float((unsigned)b_sp[128 * pi + 32 * w + 8 * (c >> 2) + 4 * b + (c & 3)] << 16);
        STAMP(4);
        {
            const LAS char* const pA2 = lds + AP + lane * 16;
            const LAS char* const pB = lp(addrB(lds0, w, lane));
            const unsigned bB[2] = {opaque(addrB(lds0, w, lane)), opaque(addrB(lds0, w, lane) + 2 * SLOT)};
            const unsigned bA[4] = {opaque(lds0 + AP + lane * 16), opaque(lds0 + AP + lane * 16 + SLOT), opaque(lds0 + AP + lane * 16 + 2 * SLOT), opaque(lds0 + AP + lane * 16 + 3 * SLOT)};
            s16x8 A0[9], A1[9], B0[2], B1[2];
#pragma unroll
            for (int a = 0; a < 9; ++a) { acc[a][0] = (f32x4){0.f, 0.f, 0.f, 0.f}; acc[a][1] = (f32x4){0.f, 0.f, 0.f, 0.f}; }
            auto step = [&](s16x8(&A)[9], s16x8(&B)[2], s16x8(&An)[9], s16x8(&Bn)[2], auto SO, auto KS, auto&& gap) {
                constexpr int so = SO.v * SLOT, ks = KS.v;
                sfor<0, 18>([&](auto I) {
                    constexpr int i = I.v, a = i >> 1, b = i & 1;
                    mfma_acc(acc[a][b], A[a], B[b]);
                    SCHED0();
                    if constexpr (i < 2) Bn[i] = lds_b128(lp(bB[SO.v >> 1]) + (SO.v & 1) * SLOT + ks * 64 + i * 512);
                    else if constexpr (i < 11) An[i - 2] = lds_b128(lp(bA[SO.v]) + (ks * 9 + i - 2) * 1024);
                    gap(I);
                    SCHED0();
                });
            };
            SCHED0();
            asm volatile("" : "+v"(acc[0][0]), "+v"(acc[0][1]), "+v"(acc[1][0]), "+v"(acc[1][1]), "+v"(acc[2][0]), "+v"(acc[2][1]), "+v"(acc[3][0]), "+v"(acc[3][1]), "+v"(acc[4][0]), "+v"(acc[4][1]), "+v"(acc[5][0]), "+v"(acc[5][1]), "+v"(acc[6][0]), "+v"(acc[6][1]), "+v"(acc[7][0]), "+v"(acc[7][1]), "+v"(acc[8][0]), "+v"(acc[8][1]));
            bar();
            STAMP(11); CSTAMP(16);
            B0[0] = lds_b128(pB + 2 * SLOT); B0[1] = lds_b128(pB + 2 * SLOT + 512);
#pragma unroll
            for (int a = 0; a < 9; ++a) A0[a] = lds_b128(pA2 + 2 * SLOT + a * 1024);
            SCHED0();
            for (int it = 0; it < 8; ++it) {
                sfor<0, 4>([&](auto U) {
                    constexpr int sl = (2 + U.v) & 3, sn = (3 + U.v) & 3;
                    step(A0, B0, A1, B1, IC<sl>{}, IC<1>{}, [&](auto I) {
                        if constexpr (I.v == BARAT) bar();
                    });
                    step(A1, B1, A0, B0, IC<sn>{}, IC<0>{}, [&](auto I) {});
                });
            }
            asm volatile("s_nop 7" ::: "memory");
            SCHED0();
            post(lds, DONE, w, lane, 50);
        }
        STAMP(5); CSTAMP(17);
        {
            const unsigned ub = WS_U + ((pi * 2 + (w >> 1)) * 16 + pj) * 18432 + (w & 1) * 2048;
            auto gate = [&](int a, int b, const u32x4& z1) {
                const f32x2 g0 = (f32x2){acc[a][b][0], acc[a][b][1]} + bias[b], g1 = (f32x2){acc[a][b][2], acc[a][b][3]} + bias[b];
                const f32x2 u0 = g0 * (f32x2){bf_lo(z1[2 * b]), bf_hi(z1[2 * b])}, u1 = g1 * (f32x2){bf_lo(z1[2 * b + 1]), bf_hi(z1[2 * b + 1])};
                return (u32x2){pk_bf16(u0.x, u0.y), pk_bf16(u1.x, u1.y)};
            };
#pragma unroll
            for (int p = 0; p < 4; ++p)
#pragma unroll
                for (int b = 0; b < 2; ++b) {
                    const u32x2 lo = gate(2 * p, b, z1[2 * p]), hi = gate(2 * p + 1, b, z1[2 * p + 1]);
                    st16_sc1(swc, lane * 16, ub + p * 4096 + b * 1024, (u32x4){lo[0], lo[1], hi[0], hi[1]});
                }
#pragma unroll
            for (int b = 0; b < 2; ++b) st8_sc1(swc, lane * 8, ub - (w & 1) * 1024 + 16384 + b * 512, gate(8, b, z1[8]));
            STAMP(10);
            SCHED0();
            load_wo(IC<0>{}, IC<10>{});
            SCHED0();
            waitv<50>();
            st4_sc1(swc, lane == 0 ? 0u : 0x80000000u, WS_UFLAG + (((pi * 2 + (w >> 1)) * 16 + pj) * 2 + (w & 1)) * 4, tag);
        }
        STAMP(6);
    } else {
        __builtin_amdgcn_s_setprio(PRODPRIO);
        const int pw = wid - 4;
        const int lane = fresh_lane(), q = lane >> 4, c = lane & 15;
        i32x4 sx = make_srd(x, S * D * 2), swi = make_srd(w_in, D * F * 2), ssp = make_srd(w_sp, S * S * 2), swo = make_srd(w_out, FH * D * 2);
        const int pos = lane >> 3, ch = lane & 7;
        const unsigned rowB = 128 * pi + 32 * pw + 8 * (((pos >> 1) & 1) + 2 * (pos & 1)) + 4 * (pos >> 2);
        const V4 vX = strided4(rowB * (D * 2) + ch * 16, D * 2), vSP = strided4(rowB * (S * 2) + ch * 16, S * 2);
        const V4 vWm = strided4((32 * (pw & 1) + 8 * ((pos & 1) + 2 * (pos >> 2)) + 4 * ((pos >> 1) & 1)) * (F * 2) + ch * 16 + colmap0(pj) * 2 + (pw >> 1) * 128, F * 2);
        const int rho = 16 * (pw & 1) + ((lane >> 1) & 15);
        const unsigned vWr = (32 * (pw >> 1) + 8 * (((rho >> 2) & 1) + 2 * (rho >> 4)) + 4 * ((rho >> 3) & 1) + (rho & 3)) * (F * 2) + (lane & 1) * 16 + colmap8(pj) * 2;
        const int rot1 = ROT1, rot2 = ROT2;
        auto k1 = [&](int kt) { const int k = kt + rot1; return k >= 9 ? k - 9 : k; };
        auto k2 = [&](int kt) { return (kt + rot2) & 31; };
        const unsigned lq = lds0 + 4 * pw * BLK;
        auto ld_x = [&](int kt, int slot) { dma16x4b(sx, vX, k1(kt) * 128, lq + slot * SLOT); };
        auto ld_w = [&](int kt, int half, unsigned at, unsigned atr) {
            const unsigned so = k1(kt) * 64 * (F * 2) + half * (FH * 2);
            dma16x4b(swi, vWm, so, lq + at);
            dma16_half(swi, vWr, so, lds0 + atr + pw * 512);
        };
        auto ld_b = [&](auto TT) { ld_w(TT.v - 9, 0, g1_main(TT.v), g1_rem(TT.v)); };
        auto ld_sp = [&](int kt, int slot) { dma16x4b(ssp, vSP, k2(kt) * 128, lq + slot * SLOT); };
        auto ld_z = [&](int kt, int slot) {
            const unsigned so = WS_Z2T + (pj * 32 + k2(kt)) * 18432;
            dma16x4_sc1(swc, lane * 16, so + pw * 4096, lds0 + slot * SLOT + AP + pw * 4096);
            dma16_half_sc1(swc, lane * 16, so + 16384 + pw * 512, lds0 + slot * SLOT + AP + 16384 + pw * 512);
        };
        auto landed = [&](int T) { post(lds, RDY, pw, lane, T + 1); };
        ld_x(0, 0); ld_w(0, 1, AP, AREM); ld_x(1, 1); ld_w(1, 1, SLOT + AP, SLOT + AREM);
        waitv<9>(); STAMP(10); bar();
        for (int t = 2; t < 9; ++t) { ld_x(t, t & 3); ld_w(t, 1, (t & 3) * SLOT + AP, (t & 3) * SLOT + AREM); waitv<9>(); bar(); }
        ld_b(IC<9>{}); waitv<5>(); bar();
        ld_b(IC<10>{});
        get_tag();
        bar();
        ld_b(IC<11>{}); ld_b(IC<12>{});
        {
            const int g3 = pj & 7, qq = (lane >> 2) & 3, pp = lane & 3, r4 = 4 * q + qq;
            const unsigned f0 = colmap0(pi) + 32 * pw, f8 = colmap8(pi), vm = pos * (D * 2) + ch * 16 + g3 * 128;
            const unsigned wb = WS_WO + (pj * 16 + pi) * 20736, sg = (2 + (pw >> 1)) * SLOT + (pw & 1) * 8192;
            const LAS char* const sp = lds + sg;
            dma16x4s<1024, 8 * D * 2>(swo, vm, f0 * (D * 2), lds0 + sg);
            dma16(swo, vm, f8 * (D * 2), lds0 + sg + 4096);
            dma16(swo, vm, (f8 + 8) * (D * 2), lds0 + sg + 5120);
            dma16(swo, (lane < 32 ? f0 + lane : f8 + (lane & 15)) * (D * 2) + 1024 + g3 * 16, 0, lds0 + sg + 6144);
            waitv<0>(); landed(12);
            s16x8 fr[4];
#pragma unroll
            for (int n = 0; n < 4; ++n) {
                const int co = r4 * 128 + (32 * (n >> 1) + 8 * pp + 4 * (n & 1)) * 2;
                fr[n] = cat8(lds_tr(sp + co), lds_tr(sp + 2048 + co));
            }
            const s16x8 f4 = cat8(lds_tr(sp + 6144 + r4 * 16 + (pp & 1) * 8), lds_tr(sp + 6144 + (16 + r4) * 16 + (pp & 1) * 8));
            const s16x4 h0 = lds_tr(sp + 4096 + r4 * 128 + (32 * (pw >> 1) + 8 * pp + 4 * (pw & 1)) * 2);
            const s16x4 h1 = lds_tr(sp + 6144 + (32 + r4) * 16 + (pp & 1) * 8);
#pragma unroll
            for (int n = 0; n < 4; ++n) st16_sc1(swc, lane * 16, wb + pw * 4096 + n * 1024, __builtin_bit_cast(u32x4, fr[n]));
            const unsigned half16 = c < 8 ? (unsigned)(q * 8 + c) * 16u : 0x80000000u, half8 = c < 8 ? (unsigned)(q * 8 + c) * 8u : 0x80000000u;
            st8_sc1(swc, lane * 8, wb + 16384 + pw * 512, __builtin_bit_cast(u32x2, h0));
            st16_sc1(swc, half16, wb + 18432 + pw * 512, __builtin_bit_cast(u32x4, f4));
            st8_sc1(swc, pw == 0 ? half8 : 0x80000000u, wb + 20480, __builtin_bit_cast(u32x2, h1));
            waitv<0>();
            st4_sc1(swc, lane == 0 ? 0u : 0x80000000u, WS_WFLAG + (pj * 64 + pi * 4 + pw) * 4, tag);
        }
        STAMP(1);
        {
            u32x4 zz[9], gr[8];
            const unsigned gvo = (lane >> 5) * 2048 + (32 * pw + (lane & 31)) * 16, gso = WS_STATS + pi * 16 * 2048;
            auto fetch = [&]() {
#pragma unroll
                for (int n = 0; n < 8; ++n) asm volatile("buffer_load_dwordx4 %0, %1, %2, %3 offen sc1" : "=v"(gr[n]) : "v"(gvo), "s"(swc), "s"(gso + n * 4096) : "memory");
            };
            auto ready = [&]() {
                asm volatile("" : "+v"(gr[0]), "+v"(gr[1]), "+v"(gr[2]), "+v"(gr[3]), "+v"(gr[4]), "+v"(gr[5]), "+v"(gr[6]), "+v"(gr[7]));
                unsigned bad = 0;
#pragma unroll
                for (int n = 0; n < 8; ++n) bad |= (gr[n][1] ^ tag) | (gr[n][3] ^ tag);
                return __all(bad == 0);
            };
            bar();
#pragma unroll
            for (int n = 0; n < 9; ++n) zz[n] = *(const LAS u32x4*)stash_at(lds, pw, lane, n);
            ld_b(IC<13>{}); ld_b(IC<14>{});
            waitl0();
            asm volatile("" : "+v"(zz[0]), "+v"(zz[1]), "+v"(zz[2]), "+v"(zz[3]), "+v"(zz[4]), "+v"(zz[5]), "+v"(zz[6]), "+v"(zz[7]), "+v"(zz[8]));
            fetch();
            ld_b(IC<15>{}); ld_b(IC<16>{});
            STAMP(11);
            waitv<10>(); landed(14);
            if (!ready()) {
                const unsigned long long t0 = __builtin_amdgcn_s_memrealtime();
                for (;;) {
                    fetch(); waitv<0>();
                    if (ready() || __builtin_amdgcn_s_memrealtime() - t0 > SPIN_TICKS) break;
                }
            }
            STAMP(13);
            float s1 = 0.f, s2 = 0.f;
#pragma unroll
            for (int n = 0; n < 8; ++n) { s1 = solo(s1 + __uint_as_float(gr[n][0])); s2 = solo(s2 + __uint_as_float(gr[n][2])); }
            s1 = solo(s1 + __shfl_xor(s1, 32)); s2 = solo(s2 + __shfl_xor(s2, 32));
            const float mean = solo(s1 * (1.0f / 2304.0f)), var = fmaxf(s2 * (1.0f / 2304.0f) - mean * mean, 0.f);
            const float rinv = __builtin_amdgcn_rcpf(rbf(__builtin_amdgcn_sqrtf(rbf(rbf(var) + 1e-5f))));
            LAS char* const tb = lds + STAB + pw * 256;
            if (lane < 32) { *(LAS float*)(tb + lane * 4) = rinv; *(LAS float*)(tb + 128 + lane * 4) = -rbf(mean) * rinv; }
            waitl0();
            const f32x4 ra = *(const LAS f32x4*)(tb + q * 32), rb = *(const LAS f32x4*)(tb + q * 32 + 16);
            const f32x4 ma = *(const LAS f32x4*)(tb + 128 + q * 32), mb = *(const LAS f32x4*)(tb + 128 + q * 32 + 16);
            const float rv[8] = {ra[0], ra[1], ra[2], ra[3], rb[0], rb[1], rb[2], rb[3]}, mv[8] = {ma[0], ma[1], ma[2], ma[3], mb[0], mb[1], mb[2], mb[3]};
            const unsigned zb = WS_Z2T + (pj * 64 + pi * 4 + pw) * 9216;
            waitv<0>(); landed(16);
#pragma unroll
            for (int a = 0; a < 9; ++a) {
                u32x4 o;
#pragma unroll
                for (int k = 0; k < 4; ++k)
                    o[k] = pk_bf16(solo(__builtin_fmaf(bf_lo(zz[a][k]), rv[2 * k], mv[2 * k])), solo(__builtin_fmaf(bf_hi(zz[a][k]), rv[2 * k + 1], mv[2 * k + 1])));
                st16_sc1(swc, lane * 16, zb + a * 1024, o);
            }
            STAMP(14);
        }
        spin4(lds, DONE, lane, 10); ld_b(IC<17>{});
        waitv<5>();
        st4_sc1(swc, lane == 0 ? 0u : 0x80000000u, WS_ZFLAG + (pj * 64 + pi * 4 + pw) * 4, tag);
        STAMP(2);
        poll_flags(swc, lane * 4, WS_WFLAG + pj * 256, tag);
        landed(17);
        SCHED0();
        load_wo(IC<0>{}, IC<WEARLY>{});
        SCHED0();
        spin4(lds, DONE, lane, 15); ld_sp(0, 2); ld_sp(1, 3);
        spin4(lds, DONE, lane, 17); ld_sp(2, 0); ld_sp(3, 1);
        STAMP(20);
        poll_flags(swc, lane * 4, WS_ZFLAG + pj * 256, tag);
        STAMP(3);
        ld_z(0, 2); ld_z(1, 3); ld_z(2, 0);
        spin4(lds, DONE, lane, 18);
        ld_z(3, 1);
        waitv<15>(); bar();
        waitv<10>(); bar();
        waitv<5>(); bar();
        for (int t = 4; t < 32; ++t) {
            ld_z(t, (t + 2) & 3); ld_sp(t, (t + 2) & 3);
            waitv<9>(); bar();
        }
        waitv<0>(); bar();
        SCHED0();
        load_wo(IC<WEARLY>{}, IC<10>{});
        SCHED0();
        bar();
        __builtin_amdgcn_s_setprio(0);
        STAMP(5);
    }

    {
        const int v = wid, h3 = pj >> 3, g3 = pj & 7;
        const int lane = fresh_lane(), q = lane >> 4, c = lane & 15;
        const unsigned ub = WS_U + ((pi * 2 + h3) * 16 + 2 * v) * 18432;
        poll_flags(swc, (lane & 3) * 4, WS_UFLAG + ((pi * 2 + h3) * 16 + 2 * v) * 8, tag);
        STAMP(7); VSTAMP(47);
        SCHED0();
        constexpr int UW = UWIN;
        u32x4 U[UW];
        auto ldU = [&](auto G) {
            constexpr int r = G.v / 10, s = G.v % 10, pn = s / 5, p = s % 5;
            if constexpr (p < 4) U[G.v % UW] = __builtin_amdgcn_raw_buffer_load_b128(rw, lane * 16, ub + pn * 18432 + (p * 4 + r) * 1024, 16);
            else {
                const u32x2 t = __builtin_amdgcn_raw_buffer_load_b64(rw, lane * 8, ub + pn * 18432 + 16384 + r * 512, 16);
                U[G.v % UW] = (u32x4){t[0], t[1], 0u, 0u};
            }
        };
        sfor<0, UW>(ldU);
        spin4(lds, DONE, lane, 50);
        SCHED0();
        sfor<0, 4>([&](auto R) {
            constexpr int r = R.v;
            f32x4 o[5];
            sfor<0, 10>([&](auto SS) {
                constexpr int s = SS.v, g = 10 * r + s;
                const f32x4 z4 = {0.f, 0.f, 0.f, 0.f};
#pragma unroll
                for (int n = 0; n < 5; ++n) {
                    if constexpr (s % 5 < 4) o[n] = mfma32(__builtin_bit_cast(s16x8, W[s][n]), __builtin_bit_cast(s16x8, U[g % UW]), s == 0 ? z4 : o[n]);
                    else o[n] = __builtin_amdgcn_mfma_f32_16x16x16bf16_1k(__builtin_bit_cast(s16x4, (u32x2){W[s][n][0], W[s][n][1]}),
                                                                          __builtin_bit_cast(s16x4, (u32x2){U[g % UW][0], U[g % UW][1]}), o[n], 0, 0, 0);
                }
                SCHED0();
                if constexpr (g + UW < 40) ldU(IC<g + UW>{});
                SCHED0();
            });
            VSTAMP(48 + r);
            LAS char* const rb = lds + r * RED;
#pragma unroll
            for (int n = 0; n < 4; ++n) *(LAS f32x4*)(rb + (n * 8 + v) * 1024 + lane * 16) = o[n];
            if (lane < 32) *(LAS f32x4*)(rb + 32768 + v * 512 + lane * 16) = o[4];
            SCHED0();
        });
        STAMP(8);
        waitl0(); bar();
        VSTAMP(52);
        {
            const int r = v >> 1, m = v & 1;
            const LAS char* const rb = lds + r * RED;
            f32x4 t0[8], t1[8];
#pragma unroll
            for (int k = 0; k < 8; ++k) { t0[k] = *(const LAS f32x4*)(rb + (m * 16 + k) * 1024 + lane * 16); t1[k] = *(const LAS f32x4*)(rb + (m * 16 + 8 + k) * 1024 + lane * 16); }
            const f32x4 ha = *(const LAS f32x4*)(rb + 32768 + m * 2048 + lane * 16), hb = *(const LAS f32x4*)(rb + 32768 + m * 2048 + 1024 + lane * 16);
            const f32x4 s0 = ((t0[0] + t0[1]) + (t0[2] + t0[3])) + ((t0[4] + t0[5]) + (t0[6] + t0[7]));
            const f32x4 s1 = ((t1[0] + t1[1]) + (t1[2] + t1[3])) + ((t1[4] + t1[5]) + (t1[6] + t1[7]));
            const int t = 128 * pi + 64 * h3 + 32 * (r >> 1) + 8 * (c >> 2) + 4 * (r & 1) + (c & 3);
            u16* orow = out + (size_t)t * D;
            *(u32x4*)(orow + 64 * g3 + 32 * m + 8 * q) = (u32x4){pk_bf16(s0[0], s0[1]), pk_bf16(s0[2], s0[3]), pk_bf16(s1[0], s1[1]), pk_bf16(s1[2], s1[3])};
            if (m == 0) {
                const f32x4 hc = *(const LAS f32x4*)(rb + 32768 + 2048 + lane * 16), hd = *(const LAS f32x4*)(rb + 32768 + 3072 + lane * 16);
                f32x4 sh = (ha + hb) + (hc + hd);
#pragma unroll
                for (int k = 0; k < 4; ++k) sh[k] += __shfl_xor(sh[k], 32);
                if (lane < 32) *(u32x2*)(orow + 512 + 8 * g3 + 4 * q) = (u32x2){pk_bf16(sh[0], sh[1]), pk_bf16(sh[2], sh[3])};
            }
        }
        if (blockIdx.x == 0 && v == 0 && lane == 0) *(unsigned*)(ws + WS_EPOCH) = tag;
        STAMP(9);
        STAMP_FLUSH();
    }
#endif
}

extern "C" void kernel_launch(void* const* d_in, const int* in_sizes, int n_in, void* d_out, int out_size, void* d_ws, size_t ws_size,
                              hipStream_t stream) {
    (void)in_sizes; (void)n_in; (void)out_size; (void)ws_size;
    (void)hipFuncSetAttribute((const void*)gmlp_kernel, hipFuncAttributeMaxDynamicSharedMemorySize, LDS_BYTES);
    gmlp_kernel<<<256, 512, LDS_BYTES, stream>>>((const u16*)d_in[0], (const u16*)d_in[1], (const u16*)d_in[2], (const u16*)d_in[3],
                                                 (const u16*)d_in[4], (u16*)d_out, (char*)d_ws);
}
```

```cpp
#include <hip/hip_runtime.h>
#include <hip/hip_bf16.h>
#include <stdint.h>
#include <utility>

typedef unsigned short u16;
typedef short s16x4 __attribute__((ext_vector_type(4)));
typedef short s16x8 __attribute__((ext_vector_type(8)));
typedef __bf16 bf16x2 __attribute__((ext_vector_type(2)));
typedef __bf16 bf16x8 __attribute__((ext_vector_type(8)));
typedef float f32x2 __attribute__((ext_vector_type(2)));
typedef float f32x4 __attribute__((ext_vector_type(4)));
typedef unsigned u32x2 __attribute__((ext_vector_type(2)));
typedef unsigned u32x4 __attribute__((ext_vector_type(4)));
typedef int i32x4 __attribute__((ext_vector_type(4)));
#define LAS __attribute__((address_space(3)))
#define DEVI __device__ __forceinline__

constexpr int S = 2048, D = 576, F = 4608, FH = 2304;
constexpr int BLK = 1056;
constexpr int AP = 16 * BLK;
constexpr int AREM = AP + 16 * BLK;
constexpr int SLOT = AREM + 2048;
constexpr int MISC = 4 * SLOT;
constexpr int RED = 36864;
constexpr int RDY = MISC + 16384;
constexpr int DONE = RDY + 256;
constexpr int HOFF = RDY + 512;
constexpr int HOF2 = RDY + 1024;
constexpr int STAB = MISC + 18432;
constexpr int SCRAP = MISC + 19456;
constexpr int STASH8 = MISC + 4096;
constexpr int REMX = MISC + 8192;
constexpr int LDS_BYTES = 163840;
constexpr unsigned WS_EPOCH = 0;
constexpr unsigned WS_ZFLAG = 4096;
constexpr unsigned WS_UFLAG = 8192;
constexpr unsigned WS_WFLAG = 12288;
constexpr unsigned WS_STATS = 16384;
constexpr unsigned WS_Z2T = 2u << 20;
constexpr unsigned WS_U = WS_Z2T + 16u * 64 * 9216;
constexpr unsigned WS_WO = WS_U + 512u * 18432;
constexpr unsigned WS_END = WS_WO + 256u * 20736;
constexpr unsigned WS_DBG = 32u << 20;

#define SLEEPN 1
#define ZLD "sc1"
#define STB "sc1"
template <int N> struct IC { static constexpr int v = N; };
template <int B, class Fn, int... I> DEVI void sfor_(Fn& f, std::integer_sequence<int, I...>) { (f(IC<B + I>{}), ...); }
template <int B, int E, class Fn> DEVI void sfor(Fn&& f) { sfor_<B>(f, std::make_integer_sequence<int, E - B>{}); }

DEVI i32x4 make_srd(const void* p, unsigned bytes) {
    i32x4 r; const unsigned long long a = (unsigned long long)p;
    r.x = (int)(unsigned)a; r.y = (int)((unsigned)(a >> 32) & 0xffffu); r.z = (int)bytes; r.w = 0x00020000;
    return r;
}
DEVI void dma16(i32x4 srd, unsigned voff, unsigned soff, unsigned lds) {
    asm volatile("s_mov_b32 m0, %3\n\ts_nop 0\n\tbuffer_load_dwordx4 %0, %1, %2 offen lds" ::"v"(voff), "s"(srd), "s"(soff), "s"(lds) : "memory");
}
DEVI void dma16_sc1(i32x4 srd, unsigned voff, unsigned soff, unsigned lds) {
    asm volatile("s_mov_b32 m0, %3\n\ts_nop 0\n\tbuffer_load_dwordx4 %0, %1, %2 offen " ZLD " lds" ::"v"(voff), "s"(srd), "s"(soff), "s"(lds) : "memory");
}
DEVI void dma16x4_sc1(i32x4 srd, unsigned voff, unsigned soff, unsigned lds) {
    asm volatile("s_mov_b32 m0, %3\n\ts_nop 0\n\tbuffer_load_dwordx4 %0, %1, %2 offen " ZLD " lds\n\t"
                 "buffer_load_dwordx4 %0, %1, %2 offen offset:1024 " ZLD " lds\n\t"
                 "buffer_load_dwordx4 %0, %1, %2 offen offset:2048 " ZLD " lds\n\t"
                 "buffer_load_dwordx4 %0, %1, %2 offen offset:3072 " ZLD " lds" ::"v"(voff), "s"(srd), "s"(soff), "s"(lds) : "memory");
}
struct V4 { unsigned v[4]; };
DEVI V4 strided4(unsigned base, unsigned stride) { return V4{{base, base + stride - BLK, base + 2 * (stride - BLK), base + 3 * (stride - BLK)}}; }
DEVI void dma16x4b(i32x4 srd, const V4& v, unsigned soff, unsigned lds) {
    asm volatile("s_mov_b32 m0, %6\n\ts_nop 0\n\tbuffer_load_dwordx4 %0, %4, %5 offen lds\n\t"
                 "buffer_load_dwordx4 %1, %4, %5 offen offset:1056 lds\n\t"
                 "buffer_load_dwordx4 %2, %4, %5 offen offset:2112 lds\n\t"
                 "buffer_load_dwordx4 %3, %4, %5 offen offset:3168 lds"
                 ::"v"(v.v[0]), "v"(v.v[1]), "v"(v.v[2]), "v"(v.v[3]), "s"(srd), "s"(soff), "s"(lds) : "memory");
}
template <int LS, int MS> DEVI void dma16x4s(i32x4 srd, unsigned voff, unsigned soff, unsigned lds) {
    unsigned t;
    asm volatile("s_mov_b32 m0, %4\n\ts_add_u32 %0, %3, %6\n\tbuffer_load_dwordx4 %1, %2, %3 offen lds\n\t"
                 "s_add_u32 m0, m0, %5\n\ts_nop 0\n\tbuffer_load_dwordx4 %1, %2, %0 offen lds\n\t"
                 "s_add_u32 m0, m0, %5\n\ts_add_u32 %0, %0, %6\n\tbuffer_load_dwordx4 %1, %2, %0 offen lds\n\t"
                 "s_add_u32 m0, m0, %5\n\ts_add_u32 %0, %0, %6\n\tbuffer_load_dwordx4 %1, %2, %0 offen lds"
                 : "=&s"(t) : "v"(voff), "s"(srd), "s"(soff), "s"(lds), "i"(LS), "i"(MS) : "memory", "scc");
}
DEVI void dma16_half(i32x4 srd, unsigned voff, unsigned soff, unsigned lds) {
    asm volatile("s_mov_b32 m0, %3\n\ts_mov_b64 exec, 0xffffffff\n\tbuffer_load_dwordx4 %0, %1, %2 offen lds\n\ts_mov_b64 exec, -1" ::"v"(voff), "s"(srd), "s"(soff), "s"(lds) : "memory");
}
DEVI void dma16_half_sc1(i32x4 srd, unsigned voff, unsigned soff, unsigned lds) {
    asm volatile("s_mov_b32 m0, %3\n\ts_mov_b64 exec, 0xffffffff\n\tbuffer_load_dwordx4 %0, %1, %2 offen " ZLD " lds\n\ts_mov_b64 exec, -1" ::"v"(voff), "s"(srd), "s"(soff), "s"(lds) : "memory");
}
DEVI void touch(i32x4 srd, unsigned voff, unsigned soff, unsigned lds) {
    asm volatile("s_mov_b32 m0, %3\n\ts_nop 0\n\tbuffer_load_dword %0, %1, %2 offen lds" ::"v"(voff), "s"(srd), "s"(soff), "s"(lds) : "memory");
}
DEVI void mfma_acc(f32x4& c, s16x8 a, s16x8 b) { asm volatile("v_mfma_f32_16x16x32_bf16 %0, %1, %2, %0" : "+v"(c) : "v"(a), "v"(b)); }
DEVI void mfma_new(f32x4& c, s16x8 a, s16x8 b) { asm volatile("v_mfma_f32_16x16x32_bf16 %0, %1, %2, 0" : "=v"(c) : "v"(a), "v"(b)); }
DEVI void st16_sc1(i32x4 srd, unsigned voff, unsigned soff, u32x4 v) {
    asm volatile("buffer_store_dwordx4 %0, %1, %2, %3 offen " STB "\n\ts_nop 1" ::"v"(v), "v"(voff), "s"(srd), "s"(soff) : "memory");
}
DEVI void st8_sc1(i32x4 srd, unsigned voff, unsigned soff, u32x2 v) {
    asm volatile("buffer_store_dwordx2 %0, %1, %2, %3 offen " STB "\n\ts_nop 1" ::"v"(v), "v"(voff), "s"(srd), "s"(soff) : "memory");
}
DEVI void st4_sc1(i32x4 srd, unsigned voff, unsigned soff, unsigned v) {
    asm volatile("buffer_store_dword %0, %1, %2, %3 offen sc1\n\ts_nop 1" ::"v"(v), "v"(voff), "s"(srd), "s"(soff) : "memory");
}
template <int N> DEVI void waitv() { asm volatile("s_waitcnt vmcnt(%0)" ::"i"(N) : "memory"); }
DEVI void waitl0() { asm volatile("s_waitcnt lgkmcnt(0)" ::: "memory"); }
DEVI void bar() { asm volatile("s_barrier" ::: "memory"); }
#define SCHED0() __builtin_amdgcn_sched_barrier(0)

DEVI unsigned pk_bf16(float lo, float hi) { f32x2 x = {lo, hi}; return __builtin_bit_cast(unsigned, __builtin_convertvector(x, bf16x2)); }
DEVI float bf_lo(unsigned p) { return __uint_as_float(p << 16); }
DEVI float bf_hi(unsigned p) { return __uint_as_float(p & 0xffff0000u); }
DEVI float rbf(float x) { return bf_lo(pk_bf16(x, x)); }
DEVI f32x2 gelu2(f32x2 h) {
    const float c1 = -2.302208198f, c2 = -0.1029432397f;
    f32x2 t = (h * h) * c2 + c1;
    t = t * h;
    f32x2 e = {__builtin_amdgcn_exp2f(t.x), __builtin_amdgcn_exp2f(t.y)};
    e = e + 1.0f;
    f32x2 r = {__builtin_amdgcn_rcpf(e.x), __builtin_amdgcn_rcpf(e.y)};
    return h * r;
}
DEVI float solo(float x) { asm("" : "+v"(x)); return x; }
DEVI f32x4 add4(f32x4 a, f32x4 b) { return (f32x4){solo(a[0] + b[0]), solo(a[1] + b[1]), solo(a[2] + b[2]), solo(a[3] + b[3])}; }
DEVI float gelu1(float h) {
    const float t = __builtin_fmaf(h * h, -0.1029432397f, -2.302208198f) * h;
    return solo(h * __builtin_amdgcn_rcpf(__builtin_amdgcn_exp2f(t) + 1.0f));
}
DEVI unsigned gelu_pk(unsigned hp) { return pk_bf16(gelu1(bf_lo(hp)), gelu1(bf_hi(hp))); }
template <int CTRL> DEVI float dpp_add(float x) {
    return x + __int_as_float(__builtin_amdgcn_update_dpp(0, __float_as_int(x), CTRL, 0xf, 0xf, false));
}
DEVI float row16_sum(float x) { x = dpp_add<0x128>(x); x = dpp_add<0x124>(x); x = dpp_add<0x122>(x); x = dpp_add<0x121>(x); return x; }
DEVI void publish_stats(const f32x2 (&s1)[4], const f32x2 (&s2)[4], i32x4 swc, unsigned soff, int w, int lane, unsigned tag) {
    const int q = lane >> 4, c = lane & 15;
    float a = row16_sum(s1[0].x), b = row16_sum(s2[0].x);
#pragma unroll
    for (int r = 1; r < 8; ++r) {
        const float ta = row16_sum(s1[r >> 1][r & 1]), tb = row16_sum(s2[r >> 1][r & 1]);
        a = (c == r) ? ta : a; b = (c == r) ? tb : b;
    }
    asm volatile("buffer_store_dwordx4 %0, %1, %2, %3 offen sc1\n\ts_nop 1" ::"v"((u32x4){__float_as_uint(a), tag, __float_as_uint(b), tag}), "v"(c < 8 ? (unsigned)(32 * w + 8 * q + c) * 16u : 0x80000000u), "s"(swc), "s"(soff) : "memory");
}
DEVI f32x4 mfma32(s16x8 a, s16x8 b, f32x4 c) {
    return __builtin_amdgcn_mfma_f32_16x16x32_bf16(__builtin_bit_cast(bf16x8, a), __builtin_bit_cast(bf16x8, b), c, 0, 0, 0);
}
DEVI s16x8 lds_b128(const LAS char* p) { return *(const LAS s16x8*)p; }
DEVI s16x4 lds_tr(const LAS char* p) { return __builtin_amdgcn_ds_read_tr16_b64_v4i16((LAS s16x4*)p); }
DEVI s16x8 cat8(s16x4 lo, s16x4 hi) { return __builtin_shufflevector(lo, hi, 0, 1, 2, 3, 4, 5, 6, 7); }
DEVI int colmap8(int j) { return 576 * (j >> 2) + 512 + 16 * (j & 3); }
DEVI int colmap0(int j) { return 576 * (j >> 2) + 128 * (j & 3); }
DEVI int fresh_lane() { int l; asm volatile("v_mbcnt_lo_u32_b32 %0, -1, 0\n\tv_mbcnt_hi_u32_b32 %0, -1, %0" : "=v"(l)); return l; }
template <int DLT> DEVI void bump(unsigned& a) { asm volatile("v_add_u32 %0, %1, %0" : "+v"(a) : "i"(DLT)); }
DEVI unsigned opaque(unsigned x) { asm volatile("" : "+v"(x)); return x; }
DEVI unsigned long long lanes_below(unsigned v, unsigned need) { unsigned long long m; asm volatile("v_cmp_gt_u32 %0, %1, %2" : "=s"(m) : "s"(need), "v"(v)); return m; }
DEVI const LAS char* lp(unsigned a) { return (const LAS char*)(size_t)a; }
DEVI unsigned addrB(unsigned lds0, int w, int lane) { const int q = lane >> 4, c = lane & 15; return lds0 + (4 * w + (c & 3)) * BLK + ((c >> 3) + 2 * ((c >> 2) & 1)) * 128 + q * 16; }
constexpr int g1_main(int T) { return (T >= 13 && T <= 16) ? ((T - 11) & 3) * SLOT : (T & 3) * SLOT + AP; }
constexpr int g1_rem(int T) { return (T >= 13 && T <= 16) ? REMX + (T - 13) * 2048 : (T & 3) * SLOT + AREM; }
DEVI int e2b(int e) { return e; }
DEVI LAS char* stash_at(LAS char* lds, int w, int lane, int n) {
    return n < 4 ? lds + (4 * w + n) * BLK + lane * 16 : n < 8 ? lds + SLOT + (4 * w + n - 4) * BLK + lane * 16 : lds + STASH8 + w * 1024 + lane * 16;
}
DEVI void post(LAS char* lds, int at, int w, int lane, unsigned v) { *(volatile LAS unsigned*)(lds + at + w * 64 + (lane & 15) * 4) = v; }
DEVI unsigned peek4(LAS char* lds, int at, int lane) { return *(volatile LAS unsigned*)(lds + at + lane * 4); }
DEVI void spin4(LAS char* lds, int at, int lane, unsigned need) {
#pragma nounroll
    for (int n = 0; n < (1 << 18); ++n) { if (__all(peek4(lds, at, lane) >= need)) break; __builtin_amdgcn_s_sleep(SLEEPN); }
    asm volatile("" ::: "memory");
}
DEVI void spin1(LAS char* lds, int at, int w, unsigned need) {
#pragma nounroll
    for (int n = 0; n < (1 << 18); ++n) { if (__all(*(volatile LAS unsigned*)(lds + at + w * 64) >= need)) break; __builtin_amdgcn_s_sleep(SLEEPN); }
    asm volatile("" ::: "memory");
}
constexpr unsigned long long SPIN_TICKS = 400000ull;

DEVI void poll_flags(i32x4 srd, unsigned voff, unsigned soff, unsigned tag) {
    const unsigned long long t0 = __builtin_amdgcn_s_memrealtime();
    for (;;) {
        unsigned f;
        asm volatile("buffer_load_dword %0, %1, %2, %3 offen sc1\n\ts_waitcnt vmcnt(0)" : "=v"(f) : "v"(voff), "s"(srd), "s"(soff) : "memory");
        if (__all(f == tag) || __builtin_amdgcn_s_memrealtime() - t0 > SPIN_TICKS) break;
        __builtin_amdgcn_s_sleep(1);
    }
}
#define STAMP(k) do {} while (0)
#define CSTAMP(k) do {} while (0)
#define COUNT(k) do {} while (0)
#define VSTAMP(idx) do {} while (0)
#define STAMP_DECL do {} while (0)
#define STAMP_FLUSH() do {} while (0)

#define DMAUNR 4
#define WEARLY 4
#define UWIN 8
#define PACE() do {} while (0)
#define SEENAT 0
#define DONEAT 8
#define G1SEEN 9
#define G2BASE
#define BARAT 17
#define WCPRE 4
#define CODEPF 46000
#define TOUCHSP 1
#define TOUCHC 0
#define G1WIN 6
#define PRODPRIO 2
#define ROT1 ((jl + 2 * il) % 9)
#define ROT2 (4 * jl)
#define RPM 1
#define WIN2 1
#define PHASE2 0
__global__ __launch_bounds__(512, 2) void gmlp_kernel(const u16* __restrict__ x, const u16* __restrict__ w_in, const u16* __restrict__ w_sp,
                                                      const u16* __restrict__ b_sp, const u16* __restrict__ w_out, u16* __restrict__ out,
                                                      char* __restrict__ ws) {
#if defined(__HIP_DEVICE_COMPILE__)
    extern __shared__ __attribute__((aligned(16))) char shm_[];
    LAS char* const lds = (LAS char*)shm_;
    const unsigned lds0 = (unsigned)(size_t)lds;
    unsigned long long pc;
    asm volatile("s_getpc_b64 %0" : "=s"(pc));
    asm volatile("" ::"s"(x), "s"(w_in), "s"(w_sp), "s"(w_out), "s"(ws));
    const int wid = __builtin_amdgcn_readfirstlane(threadIdx.x >> 6);
    const int xcd = blockIdx.x & 7, loc = blockIdx.x >> 3;
    const int il = loc & 3, jl = loc >> 2;
    const int pi = 4 * (xcd >> 1) + il, pj = 8 * (xcd & 1) + jl;
    unsigned tag;
    auto get_tag = [&]() {
        asm volatile("s_load_dword %0, %1, 0x0\n\ts_waitcnt lgkmcnt(0)" : "=s"(tag) : "s"(ws) : "memory");
        tag += 1u;
    };
    i32x4 swc = make_srd(ws, WS_END);
    const __amdgpu_buffer_rsrc_t rw = __builtin_amdgcn_make_buffer_rsrc((void*)ws, 0, WS_END, 0x00020000);
    STAMP_DECL;
    STAMP(0);
    if (wid == 0) { const int l = fresh_lane(); *(LAS u32x4*)(lds + RDY + l * 16) = (u32x4){0u, 0u, 0u, 0u}; *(LAS unsigned*)(lds + HOF2 + l * 4) = 0u; waitl0(); }
    u32x4 W[10][5];
    auto load_wo = [&](auto S0, auto S1) {
        const int lane = fresh_lane(), hl = (lane >> 4) * 8 + (lane & 7);
        const unsigned wb = WS_WO + (pj * 16 + 2 * wid) * 20736;
#pragma unroll
        for (int s = S0.v; s < S1.v; ++s)
#pragma unroll
            for (int n = 0; n < 5; ++n) {
                const unsigned pb = wb + (s / 5) * 20736;
                if (s % 5 < 4) W[s][n] = __builtin_amdgcn_raw_buffer_load_b128(rw, (n < 4 ? lane : hl) * 16, n < 4 ? pb + (s % 5) * 4096 + n * 1024 : pb + 18432 + (s % 5) * 512, 16);
                else {
                    const u32x2 t = __builtin_amdgcn_raw_buffer_load_b64(rw, (n < 4 ? lane : hl) * 8, n < 4 ? pb + 16384 + n * 512 : pb + 20480, 16);
                    W[s][n] = (u32x4){t[0], t[1], 0u, 0u};
                }
            }
    };

    if (wid < 4) {
        const int w = wid;
        f32x4 acc[9][2];
        {
            unsigned aB, aM, aR;
            unsigned seen = 0, aRdy, aDone, val;
            unsigned long long lag = 0;
            get_tag();
            if (loc < 2) {
                const i32x4 sc = make_srd((const void*)(pc & ~255ull), CODEPF);
                const unsigned vo = (unsigned)(fresh_lane() + 64 * (w + 4 * loc)) * 128u;
                asm volatile("s_mov_b32 m0, %2\n\ts_nop 0\n\tbuffer_load_dword %0, %1, 0 offen lds" ::"v"(vo), "s"(sc), "s"(lds0 + SCRAP) : "memory");
            }
            {
                const int lane = fresh_lane(), q = lane >> 4, qq = (lane >> 2) & 3, pp = lane & 3;
                aB = addrB(lds0, w, lane);
                aRdy = opaque(lds0 + RDY + lane * 4); aDone = opaque(lds0 + DONE + w * 64 + (lane & 15) * 4);
                aM = lds0 + AP + qq * BLK + ((q & 1) + 4 * (q >> 1)) * 128 + pp * 8;
                aR = lds0 + AREM + (qq + 4 * (q & 1) + 16 * (q >> 1)) * 32 + pp * 8;
            }
            auto touch_sp = [&]() {
                touch(make_srd(w_sp, S * S * 2), fresh_lane() * 128, (blockIdx.x * 4 + w) * 8192, lds0 + SCRAP);
            };
            if (TOUCHC == 1) touch_sp();
            s16x8 X[18][2];
            constexpr int WIN = G1WIN;
            s16x4 Wf[WIN][2];
            auto pass = [&](auto PA) {
                constexpr bool first = PA.v == 0;
                constexpr int T0 = first ? 0 : 9;
                auto rd = [&](auto N, auto H) {
                    constexpr int n = N.v, h = H.v, kt = n / 18, ks = (n % 18) / 9, a = n % 9;
                    constexpr int T = T0 + kt;
                    if constexpr (n % 18 == 0 && h == 0) {
                        if constexpr (first) { bar(); if constexpr (TOUCHC == 2 && (kt & 1) == 1) { if (w == kt / 2) touch_sp(); } if constexpr (TOUCHC == 4 && kt == 2) touch_sp(); }
                        else if (kt == 0 || __builtin_expect(lag != 0, 0)) { COUNT(14); spin4(lds, RDY, fresh_lane(), T + 1); }
                        SCHED0();
                    }
                    if constexpr (first && a == 0) X[2 * kt + ks][h] = lds_b128(lp(aB) + ks * 64 + h * 512);
                    if constexpr (a < 8) Wf[n % WIN][h] = lds_tr(lp(aM) + (a >> 2) * 8 * BLK + ks * 4 * BLK + (a & 3) * 32 + h * 256);
                    else Wf[n % WIN][h] = lds_tr(lp(aR) + ks * 1024 + h * 256);
                    SCHED0();
                    if constexpr (!first && n % 18 == G1SEEN && h == 0 && kt < 8) seen = *(volatile LAS unsigned*)(size_t)aRdy;
                    if constexpr (!first && n % 18 == 17 && h == 0 && kt < 8) lag = lanes_below(seen, T0 + kt + 2);
                    if constexpr (first && n % 18 == 10 && h == 0 && kt < 8) bump<(((T + 1) & 3) - (T & 3)) * SLOT>(aB);
                    if constexpr (n % 18 == 16 && h == 1 && T < 17) bump<g1_main(T + 1) - g1_main(T)>(aM);
                    if constexpr (n % 18 == 1 && h == 0 && T > 0) bump<g1_rem(T) - g1_rem(T - 1)>(aR);
                };
                sfor<0, WIN - 1>([&](auto N) { rd(N, IC<0>{}); rd(N, IC<1>{}); });
                sfor<0, 324>([&](auto M) {
                    constexpr int n = M.v >> 1, b = M.v & 1, kx = n / 9, a = n % 9;
                    const s16x8 wf = cat8(Wf[n % WIN][0], Wf[n % WIN][1]);
                    if constexpr (first) { if constexpr (kx == 0) mfma_new(acc[a][b], X[kx][b], wf); else mfma_acc(acc[a][b], X[kx][b], wf); }
                    else { if constexpr (kx == 0) mfma_new(acc[a][b], wf, X[kx][b]); else mfma_acc(acc[a][b], wf, X[kx][b]); }
                    SCHED0();
                    if constexpr (n + WIN - 1 < 162) rd(IC<n + WIN - 1>{}, IC<b>{});
                    if constexpr (!first && n % 18 == 16 && b == 1) val = opaque(T0 + n / 18 + 1);
                    if constexpr (!first && n % 18 == 17 && b == 0) *(volatile LAS unsigned*)(size_t)aDone = val;
                    SCHED0();
                });
                asm volatile("s_nop 7" ::: "memory");
                SCHED0();
            };
            CSTAMP(18);
            pass(IC<0>{});
            bar();
            STAMP(1); CSTAMP(19);
            {
                const int lane = fresh_lane();
                f32x2 s1[4] = {}, s2[4] = {};
#pragma unroll
                for (int a = 0; a < 9; ++a) {
                    u32x4 z;
#pragma unroll
                    for (int k = 0; k < 4; ++k) {
                        const f32x2 y = gelu2((f32x2){acc[a][k >> 1][2 * (k & 1)], acc[a][k >> 1][2 * (k & 1) + 1]});
                        s1[k] += y; s2[k] += y * y;
                        z[k] = pk_bf16(y.x, y.y);
                    }
                    *(LAS u32x4*)stash_at(lds, w, lane, a) = z;
                    SCHED0();
                }
                publish_stats(s1, s2, swc, WS_STATS + (pi * 16 + pj) * 2048, w, lane, tag);
                waitl0();
                post(lds, HOFF, w, lane, 1);
            }
            STAMP(2);
            if (TOUCHC == 3) touch_sp();
            CSTAMP(20);
            pass(IC<1>{});
            CSTAMP(21);
        }
        STAMP(3);
        const int lane = fresh_lane(), c = lane & 15;
        u32x4 z1[9];
#pragma unroll
        for (int a = 0; a < 9; ++a) {
#pragma unroll
            for (int k = 0; k < 4; ++k) {
                const f32x2 y = gelu2((f32x2){acc[a][k >> 1][2 * (k & 1)], acc[a][k >> 1][2 * (k & 1) + 1]});
                z1[a][k] = pk_bf16(y.x, y.y);
            }
            asm volatile("" : "+v"(z1[a]));
        }
        float bias[2];
#pragma unroll
        for (int b = 0; b < 2; ++b) bias[b] = __uint_as_float((unsigned)b_sp[128 * pi + 32 * w + 8 * (c >> 2) + 4 * b + (c & 3)] << 16);
        STAMP(4);
        {
            const LAS char* const pA2 = lds + AP + lane * 16;
            const LAS char* const pB = lp(addrB(lds0, w, lane));
            const unsigned bB[2] = {opaque(addrB(lds0, w, lane)), opaque(addrB(lds0, w, lane) + 2 * SLOT)};
            const unsigned bA[4] = {opaque(lds0 + AP + lane * 16), opaque(lds0 + AP + lane * 16 + SLOT), opaque(lds0 + AP + lane * 16 + 2 * SLOT), opaque(lds0 + AP + lane * 16 + 3 * SLOT)};
            s16x8 A0[9], A1[9], B0[2], B1[2];
#pragma unroll
            for (int a = 0; a < 9; ++a) { acc[a][0] = (f32x4){0.f, 0.f, 0.f, 0.f}; acc[a][1] = (f32x4){0.f, 0.f, 0.f, 0.f}; }
            auto step = [&](s16x8(&A)[9], s16x8(&B)[2], s16x8(&An)[9], s16x8(&Bn)[2], auto SO, auto KS, auto&& gap) {
                constexpr int so = SO.v * SLOT, ks = KS.v;
                sfor<0, 18>([&](auto I) {
                    constexpr int i = I.v, a = i >> 1, b = i & 1;
                    mfma_acc(acc[a][b], A[a], B[b]);
                    SCHED0();
                    if constexpr (i < 2) Bn[i] = lds_b128(lp(bB[SO.v >> 1]) + (SO.v & 1) * SLOT + ks * 64 + i * 512);
                    else if constexpr (i < 11) An[i - 2] = lds_b128(lp(bA[SO.v]) + (ks * 9 + i - 2) * 1024);
                    gap(I);
                    SCHED0();
                });
            };
            SCHED0();
            asm volatile("" : "+v"(acc[0][0]), "+v"(acc[0][1]), "+v"(acc[1][0]), "+v"(acc[1][1]), "+v"(acc[2][0]), "+v"(acc[2][1]), "+v"(acc[3][0]), "+v"(acc[3][1]), "+v"(acc[4][0]), "+v"(acc[4][1]), "+v"(acc[5][0]), "+v"(acc[5][1]), "+v"(acc[6][0]), "+v"(acc[6][1]), "+v"(acc[7][0]), "+v"(acc[7][1]), "+v"(acc[8][0]), "+v"(acc[8][1]));
            bar();
            STAMP(11); CSTAMP(16);
            B0[0] = lds_b128(pB + 2 * SLOT); B0[1] = lds_b128(pB + 2 * SLOT + 512);
#pragma unroll
            for (int a = 0; a < 9; ++a) A0[a] = lds_b128(pA2 + 2 * SLOT + a * 1024);
            SCHED0();
            for (int it = 0; it < 8; ++it) {
                sfor<0, 4>([&](auto U) {
                    constexpr int sl = (2 + U.v) & 3, sn = (3 + U.v) & 3;
                    step(A0, B0, A1, B1, IC<sl>{}, IC<1>{}, [&](auto I) {
                        if constexpr (I.v == BARAT) bar();
                    });
                    step(A1, B1, A0, B0, IC<sn>{}, IC<0>{}, [&](auto I) {});
                });
            }
            asm volatile("s_nop 7" ::: "memory");
            SCHED0();
        }
        STAMP(5); CSTAMP(17);
        {
            const unsigned ub = WS_U + ((pi * 2 + (w >> 1)) * 16 + pj) * 18432 + (w & 1) * 2048;
            auto gate = [&](int a, int b, const u32x4& z1) {
                const f32x2 g0 = (f32x2){acc[a][b][0], acc[a][b][1]} + bias[b], g1 = (f32x2){acc[a][b][2], acc[a][b][3]} + bias[b];
                const f32x2 u0 = g0 * (f32x2){bf_lo(z1[2 * b]), bf_hi(z1[2 * b])}, u1 = g1 * (f32x2){bf_lo(z1[2 * b + 1]), bf_hi(z1[2 * b + 1])};
                return (u32x2){pk_bf16(u0.x, u0.y), pk_bf16(u1.x, u1.y)};
            };
#pragma unroll
            for (int p = 0; p < 4; ++p)
#pragma unroll
                for (int b = 0; b < 2; ++b) {
                    const u32x2 lo = gate(2 * p, b, z1[2 * p]), hi = gate(2 * p + 1, b, z1[2 * p + 1]);
                    st16_sc1(swc, lane * 16, ub + p * 4096 + b * 1024, (u32x4){lo[0], lo[1], hi[0], hi[1]});
                }
#pragma unroll
            for (int b = 0; b < 2; ++b) st8_sc1(swc, lane * 8, ub - (w & 1) * 1024 + 16384 + b * 512, gate(8, b, z1[8]));
            post(lds, DONE, w, lane, 51);
            STAMP(10);
            SCHED0();
            load_wo(IC<0>{}, IC<WCPRE>{});
            SCHED0();
            waitv<5 * WCPRE>();
            st4_sc1(swc, lane == 0 ? 0u : 0x80000000u, WS_UFLAG + (((pi * 2 + (w >> 1)) * 16 + pj) * 2 + (w & 1)) * 4, tag);
            SCHED0();
            load_wo(IC<WCPRE>{}, IC<10>{});
            SCHED0();
        }
        STAMP(6);
    } else {
        __builtin_amdgcn_s_setprio(PRODPRIO);
        const int pw = wid - 4;
        const int lane = fresh_lane(), q = lane >> 4, c = lane & 15;
        i32x4 sx = make_srd(x, S * D * 2), swi = make_srd(w_in, D * F * 2), ssp = make_srd(w_sp, S * S * 2), swo = make_srd(w_out, FH * D * 2);
        const int pos = lane >> 3, ch = lane & 7;
        const unsigned rowB = 128 * pi + 32 * pw + 8 * (((pos >> 1) & 1) + 2 * (pos & 1)) + 4 * (pos >> 2);
        const V4 vX = strided4(rowB * (D * 2) + ch * 16, D * 2), vSP = strided4(rowB * (S * 2) + ch * 16, S * 2);
        const V4 vWm = strided4((32 * (pw & 1) + 8 * ((pos & 1) + 2 * (pos >> 2)) + 4 * ((pos >> 1) & 1)) * (F * 2) + ch * 16 + colmap0(pj) * 2 + (pw >> 1) * 128, F * 2);
        const int rho = 16 * (pw & 1) + ((lane >> 1) & 15);
        const unsigned vWr = (32 * (pw >> 1) + 8 * (((rho >> 2) & 1) + 2 * (rho >> 4)) + 4 * ((rho >> 3) & 1) + (rho & 3)) * (F * 2) + (lane & 1) * 16 + colmap8(pj) * 2;
        const int rot1 = ROT1, rot2 = ROT2;
        unsigned kc = rot1;
        auto k1_next = [&]() { kc = kc == 8 ? 0 : kc + 1; asm volatile("" : "+s"(kc)); };
        auto k2 = [&](int kt) { return (kt + rot2) & 31; };
        const unsigned lq = lds0 + 4 * pw * BLK;
        auto ld_x = [&](int slot) { dma16x4b(sx, vX, kc * 128, lq + slot * SLOT); };
        auto ld_w = [&](int half, unsigned at, unsigned atr) {
            const unsigned so = kc * 64 * (F * 2) + half * (FH * 2);
            dma16x4b(swi, vWm, so, lq + at);
            dma16_half(swi, vWr, so, lds0 + atr + pw * 512);
            k1_next();
        };
        auto ld_b = [&](auto TT) { ld_w(0, g1_main(TT.v), g1_rem(TT.v)); };
        auto ld_sp = [&](int kt, int slot) { dma16x4b(ssp, vSP, k2(kt) * 128, lq + slot * SLOT); };
        auto ld_z = [&](int kt, int slot) {
            const unsigned so = WS_Z2T + (pj * 32 + k2(kt)) * 18432;
            dma16x4_sc1(swc, lane * 16, so + pw * 4096, lds0 + slot * SLOT + AP + pw * 4096);
            dma16_half_sc1(swc, lane * 16, so + 16384 + pw * 512, lds0 + slot * SLOT + AP + 16384 + pw * 512);
        };
        auto landed = [&](int T) { post(lds, RDY, pw, lane, T + 1); };
        ld_x(0); ld_w(1, AP, AREM);
#pragma nounroll
        for (int t = 1; t < 9; ++t) { ld_x(t & 3); ld_w(1, (t & 3) * SLOT + AP, (t & 3) * SLOT + AREM); waitv<9>(); if (t == 1) STAMP(10); bar(); }
        ld_b(IC<9>{}); waitv<5>(); bar();
        ld_b(IC<10>{});
        get_tag();
        bar();
        {
            const int g3 = pj & 7, qq = (lane >> 2) & 3, pp = lane & 3, r4 = 4 * q + qq;
            const unsigned f0 = colmap0(pi) + 32 * pw, f8 = colmap8(pi), vm = pos * (D * 2) + ch * 16 + g3 * 128;
            const unsigned wb = WS_WO + (pj * 16 + pi) * 20736, sg = 2 * SLOT + 4 * pw * BLK;
            const LAS char* const sp = lds + sg;
            dma16x4s<1024, 8 * D * 2>(swo, vm, f0 * (D * 2), lds0 + sg);
            dma16(swo, vm, f8 * (D * 2), lds0 + sg + SLOT);
            dma16(swo, vm, (f8 + 8) * (D * 2), lds0 + sg + SLOT + 1024);
            dma16(swo, (lane < 32 ? f0 + lane : f8 + (lane & 15)) * (D * 2) + 1024 + g3 * 16, 0, lds0 + sg + SLOT + 2048);
            ld_b(IC<11>{}); ld_b(IC<12>{});
            waitv<10>();
            s16x8 fr[4];
#pragma unroll
            for (int n = 0; n < 4; ++n) {
                const int co = r4 * 128 + (32 * (n >> 1) + 8 * pp + 4 * (n & 1)) * 2;
                fr[n] = cat8(lds_tr(sp + co), lds_tr(sp + 2048 + co));
            }
            const s16x8 f4 = cat8(lds_tr(sp + SLOT + 2048 + r4 * 16 + (pp & 1) * 8), lds_tr(sp + SLOT + 2048 + (16 + r4) * 16 + (pp & 1) * 8));
            const s16x4 h0 = lds_tr(sp + SLOT + r4 * 128 + (32 * (pw >> 1) + 8 * pp + 4 * (pw & 1)) * 2);
            const s16x4 h1 = lds_tr(sp + SLOT + 2048 + (32 + r4) * 16 + (pp & 1) * 8);
#pragma unroll
            for (int n = 0; n < 4; ++n) st16_sc1(swc, lane * 16, wb + pw * 4096 + n * 1024, __builtin_bit_cast(u32x4, fr[n]));
            const unsigned half16 = c < 8 ? (unsigned)(q * 8 + c) * 16u : 0x80000000u, half8 = c < 8 ? (unsigned)(q * 8 + c) * 8u : 0x80000000u;
            st8_sc1(swc, lane * 8, wb + 16384 + pw * 512, __builtin_bit_cast(u32x2, h0));
            st16_sc1(swc, half16, wb + 18432 + pw * 512, __builtin_bit_cast(u32x4, f4));
            st8_sc1(swc, pw == 0 ? half8 : 0x80000000u, wb + 20480, __builtin_bit_cast(u32x2, h1));
            waitv<7 + (TOUCHSP == 2 ? 2 : TOUCHSP == 3 ? 1 : 0)>(); landed(12);
        }
        STAMP(1);
        {
            u32x4 zz[9], gr[8];
            const unsigned gvo = (lane >> 5) * 2048 + (32 * pw + (lane & 31)) * 16, gso = WS_STATS + pi * 16 * 2048;
            auto fetch = [&]() {
#pragma unroll
                for (int n = 0; n < 8; ++n) asm volatile("buffer_load_dwordx4 %0, %1, %2, %3 offen sc1" : "=v"(gr[n]) : "v"(gvo), "s"(swc), "s"(gso + n * 4096) : "memory");
            };
            auto ready = [&]() {
                asm volatile("" : "+v"(gr[0]), "+v"(gr[1]), "+v"(gr[2]), "+v"(gr[3]), "+v"(gr[4]), "+v"(gr[5]), "+v"(gr[6]), "+v"(gr[7]));
                unsigned bad = 0;
#pragma unroll
                for (int n = 0; n < 8; ++n) bad |= (gr[n][1] ^ tag) | (gr[n][3] ^ tag);
                return __all(bad == 0);
            };
            spin1(lds, HOFF, pw, 1);
#pragma unroll
            for (int n = 0; n < 9; ++n) zz[n] = *(const LAS u32x4*)stash_at(lds, pw, lane, n);
            waitv<0>();
            st4_sc1(swc, lane == 0 ? 0u : 0x80000000u, WS_WFLAG + (pj * 64 + pi * 4 + pw) * 4, tag);
            ld_b(IC<13>{}); ld_b(IC<14>{});
            waitl0();
            asm volatile("" : "+v"(zz[0]), "+v"(zz[1]), "+v"(zz[2]), "+v"(zz[3]), "+v"(zz[4]), "+v"(zz[5]), "+v"(zz[6]), "+v"(zz[7]), "+v"(zz[8]));
            ld_b(IC<15>{}); ld_b(IC<16>{});
            spin4(lds, DONE, lane, 10); ld_b(IC<17>{});
            {
                const unsigned vo = (128 * pi + 32 * pw + (lane & 31)) * (S * 2) + (rot2 + (lane >> 5)) * 128;
                touch(ssp, vo, 0, lds0 + SCRAP); touch(ssp, vo, 256, lds0 + SCRAP);
            }
            waitv<17>(); landed(14);
            waitv<2>(); landed(17);
            STAMP(11);
            const unsigned long long t0 = __builtin_amdgcn_s_memrealtime();
            for (;;) {
                fetch(); waitv<0>();
                landed(17);
                if (ready() || __builtin_amdgcn_s_memrealtime() - t0 > SPIN_TICKS) break;
            }
            STAMP(13);
            float s1 = 0.f, s2 = 0.f;
#pragma unroll
            for (int n = 0; n < 8; ++n) { s1 = solo(s1 + __uint_as_float(gr[n][0])); s2 = solo(s2 + __uint_as_float(gr[n][2])); }
            s1 = solo(s1 + __shfl_xor(s1, 32)); s2 = solo(s2 + __shfl_xor(s2, 32));
            const float mean = solo(s1 * (1.0f / 2304.0f)), var = fmaxf(s2 * (1.0f / 2304.0f) - mean * mean, 0.f);
            const float rinv = __builtin_amdgcn_rcpf(rbf(__builtin_amdgcn_sqrtf(rbf(rbf(var) + 1e-5f))));
            LAS char* const tb = lds + STAB + pw * 256;
            if (lane < 32) { *(LAS float*)(tb + lane * 4) = rinv; *(LAS float*)(tb + 128 + lane * 4) = -rbf(mean) * rinv; }
            waitl0();
            const f32x4 ra = *(const LAS f32x4*)(tb + q * 32), rb = *(const LAS f32x4*)(tb + q * 32 + 16);
            const f32x4 ma = *(const LAS f32x4*)(tb + 128 + q * 32), mb = *(const LAS f32x4*)(tb + 128 + q * 32 + 16);
            const float rv[8] = {ra[0], ra[1], ra[2], ra[3], rb[0], rb[1], rb[2], rb[3]}, mv[8] = {ma[0], ma[1], ma[2], ma[3], mb[0], mb[1], mb[2], mb[3]};
            const unsigned zb = WS_Z2T + (pj * 64 + pi * 4 + pw) * 9216;
#pragma unroll
            for (int a = 0; a < 9; ++a) {
                u32x4 o;
#pragma unroll
                for (int k = 0; k < 4; ++k)
                    o[k] = pk_bf16(solo(__builtin_fmaf(bf_lo(zz[a][k]), rv[2 * k], mv[2 * k])), solo(__builtin_fmaf(bf_hi(zz[a][k]), rv[2 * k + 1], mv[2 * k + 1])));
                st16_sc1(swc, lane * 16, zb + a * 1024, o);
            }
            STAMP(14);
        }
        waitv<0>();
        st4_sc1(swc, lane == 0 ? 0u : 0x80000000u, WS_ZFLAG + (pj * 64 + pi * 4 + pw) * 4, tag);
        STAMP(2);
        poll_flags(swc, lane * 4, WS_WFLAG + pj * 256, tag);
        spin4(lds, DONE, lane, 15); ld_sp(0, 2); ld_sp(1, 3);
        spin4(lds, DONE, lane, 17); ld_sp(2, 0); ld_sp(3, 1);
        SCHED0();
        load_wo(IC<0>{}, IC<WEARLY>{});
        SCHED0();
        STAMP(20);
        poll_flags(swc, lane * 4, WS_ZFLAG + pj * 256, tag);
        STAMP(3);
        ld_z(0, 2); ld_z(1, 3); ld_z(2, 0);
        spin4(lds, DONE, lane, 18);
        ld_z(3, 1);
        waitv<15>(); bar();
        waitv<10>(); bar();
        waitv<5>(); bar();
        for (int t = 4; t < 32; ++t) {
            ld_z(t, (t + 2) & 3); ld_sp(t, (t + 2) & 3);
            waitv<9>(); bar();
        }
        SCHED0();
        load_wo(IC<WEARLY>{}, IC<10>{});
        SCHED0();
        waitv<5 * (10 - WEARLY)>(); bar();
        bar();
        __builtin_amdgcn_s_setprio(0);
        STAMP(5);
    }

    {
        const int v = wid, h3 = pj >> 3, g3 = pj & 7;
        const int lane = fresh_lane(), q = lane >> 4, c = lane & 15;
        const unsigned ub = WS_U + ((pi * 2 + h3) * 16 + 2 * v) * 18432;
        poll_flags(swc, (lane & 3) * 4, WS_UFLAG + ((pi * 2 + h3) * 16 + 2 * v) * 8, tag);
        STAMP(7); VSTAMP(47);
        SCHED0();
        constexpr int UW = UWIN;
        u32x4 U[UW];
        auto ldU = [&](auto G) {
            constexpr int r = G.v / 10, s = G.v % 10, pn = s / 5, p = s % 5;
            if constexpr (p < 4) U[G.v % UW] = __builtin_amdgcn_raw_buffer_load_b128(rw, lane * 16, ub + pn * 18432 + (p * 4 + r) * 1024, 16);
            else {
                const u32x2 t = __builtin_amdgcn_raw_buffer_load_b64(rw, lane * 8, ub + pn * 18432 + 16384 + r * 512, 16);
                U[G.v % UW] = (u32x4){t[0], t[1], 0u, 0u};
            }
        };
        sfor<0, UW>(ldU);
        spin4(lds, DONE, lane, 51);
        SCHED0();
        sfor<0, 4>([&](auto R) {
            constexpr int r = R.v;
            f32x4 o[5];
            sfor<0, 10>([&](auto SS) {
                constexpr int s = SS.v, g = 10 * r + s;
                const f32x4 z4 = {0.f, 0.f, 0.f, 0.f};
#pragma unroll
                for (int n = 0; n < 5; ++n) {
                    if constexpr (s % 5 < 4) o[n] = mfma32(__builtin_bit_cast(s16x8, W[s][n]), __builtin_bit_cast(s16x8, U[g % UW]), s == 0 ? z4 : o[n]);
                    else o[n] = __builtin_amdgcn_mfma_f32_16x16x16bf16_1k(__builtin_bit_cast(s16x4, (u32x2){W[s][n][0], W[s][n][1]}),
                                                                          __builtin_bit_cast(s16x4, (u32x2){U[g % UW][0], U[g % UW][1]}), o[n], 0, 0, 0);
                }
                SCHED0();
                if constexpr (g + UW < 40) ldU(IC<g + UW>{});
                SCHED0();
            });
            VSTAMP(48 + r);
            LAS char* const rb = lds + r * RED;
#pragma unroll
            for (int n = 0; n < 4; ++n) *(LAS f32x4*)(rb + (n * 8 + v) * 1024 + lane * 16) = o[n];
            if (lane < 32) *(LAS f32x4*)(rb + 32768 + v * 512 + lane * 16) = o[4];
            SCHED0();
        });
        STAMP(8);
        waitl0(); bar();
        VSTAMP(52);
        {
            const int r = v >> 1, m = v & 1;
            const LAS char* const rb = lds + r * RED;
            f32x4 t0[8], t1[8];
#pragma unroll
            for (int k = 0; k < 8; ++k) { t0[k] = *(const LAS f32x4*)(rb + (m * 16 + k) * 1024 + lane * 16); t1[k] = *(const LAS f32x4*)(rb + (m * 16 + 8 + k) * 1024 + lane * 16); }
            const f32x4 ha = *(const LAS f32x4*)(rb + 32768 + m * 2048 + lane * 16), hb = *(const LAS f32x4*)(rb + 32768 + m * 2048 + 1024 + lane * 16);
            const f32x4 s0 = ((t0[0] + t0[1]) + (t0[2] + t0[3])) + ((t0[4] + t0[5]) + (t0[6] + t0[7]));
            const f32x4 s1 = ((t1[0] + t1[1]) + (t1[2] + t1[3])) + ((t1[4] + t1[5]) + (t1[6] + t1[7]));
            const int t = 128 * pi + 64 * h3 + 32 * (r >> 1) + 8 * (c >> 2) + 4 * (r & 1) + (c & 3);
            u16* orow = out + (size_t)t * D;
            *(u32x4*)(orow + 64 * g3 + 32 * m + 8 * q) = (u32x4){pk_bf16(s0[0], s0[1]), pk_bf16(s0[2], s0[3]), pk_bf16(s1[0], s1[1]), pk_bf16(s1[2], s1[3])};
            if (m == 0) {
                const f32x4 hc = *(const LAS f32x4*)(rb + 32768 + 2048 + lane * 16), hd = *(const LAS f32x4*)(rb + 32768 + 3072 + lane * 16);
                f32x4 sh = (ha + hb) + (hc + hd);
#pragma unroll
                for (int k = 0; k < 4; ++k) sh[k] += __shfl_xor(sh[k], 32);
                if (lane < 32) *(u32x2*)(orow + 512 + 8 * g3 + 4 * q) = (u32x2){pk_bf16(sh[0], sh[1]), pk_bf16(sh[2], sh[3])};
            }
        }
        if (blockIdx.x == 0 && v == 0 && lane == 0) *(unsigned*)(ws + WS_EPOCH) = tag;
        STAMP(9);
        STAMP_FLUSH();
    }
#endif
}

extern "C" void kernel_launch(void* const* d_in, const int* in_sizes, int n_in, void* d_out, int out_size, void* d_ws, size_t ws_size,
                              hipStream_t stream) {
    (void)in_sizes; (void)n_in; (void)out_size; (void)ws_size;
    (void)hipFuncSetAttribute((const void*)gmlp_kernel, hipFuncAttributeMaxDynamicSharedMemorySize, LDS_BYTES);
    gmlp_kernel<<<256, 512, LDS_BYTES, stream>>>((const u16*)d_in[0], (const u16*)d_in[1], (const u16*)d_in[2], (const u16*)d_in[3],
                                                 (const u16*)d_in[4], (u16*)d_out, (char*)d_ws);
}
```

```cpp
#include <hip/hip_runtime.h>
#include <hip/hip_bf16.h>
#include <stdint.h>
#include <utility>

typedef unsigned short u16;
typedef short s16x4 __attribute__((ext_vector_type(4)));
typedef short s16x8 __attribute__((ext_vector_type(8)));
typedef __bf16 bf16x2 __attribute__((ext_vector_type(2)));
typedef __bf16 bf16x8 __attribute__((ext_vector_type(8)));
typedef float f32x2 __attribute__((ext_vector_type(2)));
typedef float f32x4 __attribute__((ext_vector_type(4)));
typedef unsigned u32x2 __attribute__((ext_vector_type(2)));
typedef unsigned u32x4 __attribute__((ext_vector_type(4)));
typedef int i32x4 __attribute__((ext_vector_type(4)));
#define LAS __attribute__((address_space(3)))
#define DEVI __device__ __forceinline__

constexpr int S = 2048, D = 576, F = 4608, FH = 2304;
constexpr int BLK = 1056;
constexpr int AP = 16 * BLK;
constexpr int AREM = AP + 16 * BLK;
constexpr int SLOT = AREM + 2048;
constexpr int MISC = 4 * SLOT;
constexpr int RED = 36864;
constexpr int RDY = MISC + 16384;
constexpr int DONE = RDY + 256;
constexpr int HOFF = RDY + 512;
constexpr int SCRAP = MISC + 19456;
constexpr int STASH8 = MISC + 4096;
constexpr int REMX = MISC + 8192;
constexpr int LDS_BYTES = 163840;
constexpr unsigned WS_EPOCH = 0;
constexpr unsigned WS_ZFLAG = 4096;
constexpr unsigned WS_UFLAG = 8192;
constexpr unsigned WS_WFLAG = 16384;
constexpr unsigned WS_STATS = 32768;
constexpr unsigned WS_Z2T = 2u << 20;
constexpr unsigned WS_U = WS_Z2T + 16u * 64 * 9216;
constexpr unsigned WS_WO = WS_U + 512u * 18432;
constexpr unsigned WS_END = WS_WO + 128u * 20736;
constexpr unsigned OOB = 0x80000000u;

template <int N> struct IC { static constexpr int v = N; };
template <int B, class Fn, int... I> DEVI void sfor_(Fn& f, std::integer_sequence<int, I...>) { (f(IC<B + I>{}), ...); }
template <int B, int E, class Fn> DEVI void sfor(Fn&& f) { sfor_<B>(f, std::make_integer_sequence<int, E - B>{}); }

DEVI i32x4 make_srd(const void* p, unsigned bytes) {
    i32x4 r; const unsigned long long a = (unsigned long long)p;
    r.x = (int)(unsigned)a; r.y = (int)((unsigned)(a >> 32) & 0xffffu); r.z = (int)bytes; r.w = 0x00020000;
    return r;
}
DEVI void dma16(i32x4 srd, unsigned voff, unsigned soff, unsigned lds) {
    asm volatile("s_mov_b32 m0, %3\n\ts_nop 0\n\tbuffer_load_dwordx4 %0, %1, %2 offen lds" ::"v"(voff), "s"(srd), "s"(soff), "s"(lds) : "memory");
}
DEVI void dma16x4_sc1(i32x4 srd, unsigned voff, unsigned soff, unsigned lds) {
    asm volatile("s_mov_b32 m0, %3\n\ts_nop 0\n\tbuffer_load_dwordx4 %0, %1, %2 offen sc1 lds\n\t"
                 "buffer_load_dwordx4 %0, %1, %2 offen offset:1024 sc1 lds\n\t"
                 "buffer_load_dwordx4 %0, %1, %2 offen offset:2048 sc1 lds\n\t"
                 "buffer_load_dwordx4 %0, %1, %2 offen offset:3072 sc1 lds" ::"v"(voff), "s"(srd), "s"(soff), "s"(lds) : "memory");
}
struct V4 { unsigned v[4]; };
DEVI V4 strided4(unsigned base, unsigned stride) { return V4{{base, base + stride - BLK, base + 2 * (stride - BLK), base + 3 * (stride - BLK)}}; }
DEVI void dma16x4b(i32x4 srd, const V4& v, unsigned soff, unsigned lds) {
    asm volatile("s_mov_b32 m0, %6\n\ts_nop 0\n\tbuffer_load_dwordx4 %0, %4, %5 offen lds\n\t"
                 "buffer_load_dwordx4 %1, %4, %5 offen offset:1056 lds\n\t"
                 "buffer_load_dwordx4 %2, %4, %5 offen offset:2112 lds\n\t"
                 "buffer_load_dwordx4 %3, %4, %5 offen offset:3168 lds"
                 ::"v"(v.v[0]), "v"(v.v[1]), "v"(v.v[2]), "v"(v.v[3]), "s"(srd), "s"(soff), "s"(lds) : "memory");
}
template <int LS, int MS> DEVI void dma16x4s(i32x4 srd, unsigned voff, unsigned soff, unsigned lds) {
    unsigned t;
    asm volatile("s_mov_b32 m0, %4\n\ts_add_u32 %0, %3, %6\n\tbuffer_load_dwordx4 %1, %2, %3 offen lds\n\t"
                 "s_add_u32 m0, m0, %5\n\ts_nop 0\n\tbuffer_load_dwordx4 %1, %2, %0 offen lds\n\t"
                 "s_add_u32 m0, m0, %5\n\ts_add_u32 %0, %0, %6\n\tbuffer_load_dwordx4 %1, %2, %0 offen lds\n\t"
                 "s_add_u32 m0, m0, %5\n\ts_add_u32 %0, %0, %6\n\tbuffer_load_dwordx4 %1, %2, %0 offen lds"
                 : "=&s"(t) : "v"(voff), "s"(srd), "s"(soff), "s"(lds), "i"(LS), "i"(MS) : "memory", "scc");
}
DEVI void dma16_half(i32x4 srd, unsigned voff, unsigned soff, unsigned lds) {
    asm volatile("s_mov_b32 m0, %3\n\ts_mov_b64 exec, 0xffffffff\n\tbuffer_load_dwordx4 %0, %1, %2 offen lds\n\ts_mov_b64 exec, -1" ::"v"(voff), "s"(srd), "s"(soff), "s"(lds) : "memory");
}
DEVI void dma16_half_sc1(i32x4 srd, unsigned voff, unsigned soff, unsigned lds) {
    asm volatile("s_mov_b32 m0, %3\n\ts_mov_b64 exec, 0xffffffff\n\tbuffer_load_dwordx4 %0, %1, %2 offen sc1 lds\n\ts_mov_b64 exec, -1" ::"v"(voff), "s"(srd), "s"(soff), "s"(lds) : "memory");
}
DEVI void touch(i32x4 srd, unsigned voff, unsigned soff, unsigned lds) {
    asm volatile("s_mov_b32 m0, %3\n\ts_nop 0\n\tbuffer_load_dword %0, %1, %2 offen lds" ::"v"(voff), "s"(srd), "s"(soff), "s"(lds) : "memory");
}
DEVI void mfma_acc(f32x4& c, s16x8 a, s16x8 b) { asm volatile("v_mfma_f32_16x16x32_bf16 %0, %1, %2, %0" : "+v"(c) : "v"(a), "v"(b)); }
DEVI void mfma_new(f32x4& c, s16x8 a, s16x8 b) { asm volatile("v_mfma_f32_16x16x32_bf16 %0, %1, %2, 0" : "=v"(c) : "v"(a), "v"(b)); }
DEVI void st16_sc1(i32x4 srd, unsigned voff, unsigned soff, u32x4 v) {
    asm volatile("buffer_store_dwordx4 %0, %1, %2, %3 offen sc1\n\ts_nop 1" ::"v"(v), "v"(voff), "s"(srd), "s"(soff) : "memory");
}
DEVI void st8_sc1(i32x4 srd, unsigned voff, unsigned soff, u32x2 v) {
    asm volatile("buffer_store_dwordx2 %0, %1, %2, %3 offen sc1\n\ts_nop 1" ::"v"(v), "v"(voff), "s"(srd), "s"(soff) : "memory");
}
DEVI void st4_sc1(i32x4 srd, unsigned voff, unsigned soff, unsigned v) {
    asm volatile("buffer_store_dword %0, %1, %2, %3 offen sc1\n\ts_nop 1" ::"v"(v), "v"(voff), "s"(srd), "s"(soff) : "memory");
}
template <int N> DEVI void waitv() { asm volatile("s_waitcnt vmcnt(%0)" ::"i"(N) : "memory"); }
DEVI void waitl0() { asm volatile("s_waitcnt lgkmcnt(0)" ::: "memory"); }
DEVI void bar() { asm volatile("s_barrier" ::: "memory"); }
#define SCHED0() __builtin_amdgcn_sched_barrier(0)

DEVI unsigned pk_bf16(float lo, float hi) { f32x2 x = {lo, hi}; return __builtin_bit_cast(unsigned, __builtin_convertvector(x, bf16x2)); }
DEVI float bf_lo(unsigned p) { return __uint_as_float(p << 16); }
DEVI float bf_hi(unsigned p) { return __uint_as_float(p & 0xffff0000u); }
DEVI float rbf(float x) { return bf_lo(pk_bf16(x, x)); }
DEVI f32x2 gelu2(f32x2 h) {
    const float c1 = -2.302208198f, c2 = -0.1029432397f;
    f32x2 t = (h * h) * c2 + c1;
    t = t * h;
    f32x2 e = {__builtin_amdgcn_exp2f(t.x), __builtin_amdgcn_exp2f(t.y)};
    e = e + 1.0f;
    f32x2 r = {__builtin_amdgcn_rcpf(e.x), __builtin_amdgcn_rcpf(e.y)};
    return h * r;
}
DEVI float solo(float x) { asm("" : "+v"(x)); return x; }
template <int CTRL> DEVI float dpp_add(float x) {
    return x + __int_as_float(__builtin_amdgcn_update_dpp(0, __float_as_int(x), CTRL, 0xf, 0xf, false));
}
template <int CTRL> DEVI float dpp_get(float x) { return __int_as_float(__builtin_amdgcn_mov_dpp(__float_as_int(x), CTRL, 0xf, 0xf, true)); }
DEVI void publish_stats(const f32x2 (&s1)[4], const f32x2 (&s2)[4], i32x4 swc, unsigned soff, int w, int lane, unsigned tag) {
    const int q = lane >> 4, c = lane & 15;
    const bool b2 = c & 4, b1 = c & 2, b0 = c & 1;
    float u[2][4], v[2][2], y[2];
#pragma unroll
    for (int e = 0; e < 2; ++e) {
        const f32x2(&x)[4] = e ? s2 : s1;
#pragma unroll
        for (int k = 0; k < 4; ++k) { const float lo = x[k >> 1][k & 1], hi = x[2 + (k >> 1)][k & 1]; u[e][k] = (b2 ? hi : lo) + dpp_get<0x141>(b2 ? lo : hi); }
#pragma unroll
        for (int k = 0; k < 2; ++k) v[e][k] = (b1 ? u[e][2 + k] : u[e][k]) + dpp_get<0x4e>(b1 ? u[e][k] : u[e][2 + k]);
        y[e] = (b0 ? v[e][1] : v[e][0]) + dpp_get<0xb1>(b0 ? v[e][0] : v[e][1]);
        y[e] = dpp_add<0x128>(y[e]);
    }
    st16_sc1(swc, c < 8 ? (unsigned)(32 * w + 8 * q + c) * 16u : OOB, soff, (u32x4){__float_as_uint(y[0]), tag, __float_as_uint(y[1]), tag});
}
DEVI f32x4 mfma32(s16x8 a, s16x8 b, f32x4 c) {
    return __builtin_amdgcn_mfma_f32_16x16x32_bf16(__builtin_bit_cast(bf16x8, a), __builtin_bit_cast(bf16x8, b), c, 0, 0, 0);
}
DEVI s16x8 lds_b128(const LAS char* p) { return *(const LAS s16x8*)p; }
DEVI s16x4 lds_tr(const LAS char* p) { return __builtin_amdgcn_ds_read_tr16_b64_v4i16((LAS s16x4*)p); }
DEVI s16x8 cat8(s16x4 lo, s16x4 hi) { return __builtin_shufflevector(lo, hi, 0, 1, 2, 3, 4, 5, 6, 7); }
DEVI int colmap8(int j) { return 576 * (j >> 2) + 512 + 16 * (j & 3); }
DEVI int colmap0(int j) { return 576 * (j >> 2) + 128 * (j & 3); }
DEVI int fresh_lane() { int l; asm volatile("v_mbcnt_lo_u32_b32 %0, -1, 0\n\tv_mbcnt_hi_u32_b32 %0, -1, %0" : "=v"(l)); return l; }
template <int DLT> DEVI void bump(unsigned& a) { asm volatile("v_add_u32 %0, %1, %0" : "+v"(a) : "i"(DLT)); }
DEVI unsigned opaque(unsigned x) { asm volatile("" : "+v"(x)); return x; }
DEVI unsigned long long lanes_below(unsigned v, unsigned need) { unsigned long long m; asm volatile("v_cmp_gt_u32 %0, %1, %2" : "=s"(m) : "s"(need), "v"(v)); return m; }
DEVI const LAS char* lp(unsigned a) { return (const LAS char*)(size_t)a; }
DEVI unsigned addrB(unsigned lds0, int w, int lane) { const int q = lane >> 4, c = lane & 15; return lds0 + (4 * w + (c & 3)) * BLK + ((c >> 3) + 2 * ((c >> 2) & 1)) * 128 + q * 16; }
constexpr int g1_main(int T) { return (T >= 13 && T <= 16) ? ((T - 11) & 3) * SLOT : (T & 3) * SLOT + AP; }
constexpr int g1_rem(int T) { return (T >= 13 && T <= 16) ? REMX + (T - 13) * 2048 : (T & 3) * SLOT + AREM; }
DEVI LAS char* stash_at(LAS char* lds, int w, int lane, int n) {
    return n < 4 ? lds + (4 * w + n) * BLK + lane * 16 : n < 8 ? lds + SLOT + (4 * w + n - 4) * BLK + lane * 16 : lds + STASH8 + w * 1024 + lane * 16;
}
DEVI void post(LAS char* lds, int at, int w, int lane, unsigned v) { *(volatile LAS unsigned*)(lds + at + w * 64 + (lane & 15) * 4) = v; }
DEVI unsigned peek4(LAS char* lds, int at, int lane) { return *(volatile LAS unsigned*)(lds + at + lane * 4); }
DEVI void spin4(LAS char* lds, int at, int lane, unsigned need) {
#pragma nounroll
    for (int n = 0; n < (1 << 22); ++n) { if (__all(peek4(lds, at, lane) >= need)) break; __builtin_amdgcn_s_sleep(1); }
    asm volatile("" ::: "memory");
}
DEVI void spin1(LAS char* lds, int at, int w, unsigned need) {
#pragma nounroll
    for (int n = 0; n < (1 << 22); ++n) { if (__all(*(volatile LAS unsigned*)(lds + at + w * 64) >= need)) break; __builtin_amdgcn_s_sleep(1); }
    asm volatile("" ::: "memory");
}
constexpr unsigned long long SPIN_TICKS = 20000000ull;

DEVI void poll_flags(i32x4 srd, unsigned voff, unsigned soff, unsigned tag) {
    const unsigned long long t0 = __builtin_amdgcn_s_memrealtime();
    for (;;) {
        unsigned f;
        asm volatile("buffer_load_dword %0, %1, %2, %3 offen sc1\n\ts_waitcnt vmcnt(0)" : "=v"(f) : "v"(voff), "s"(srd), "s"(soff) : "memory");
        if (__all(f == tag) || __builtin_amdgcn_s_memrealtime() - t0 > SPIN_TICKS) break;
        __builtin_amdgcn_s_sleep(1);
    }
}

constexpr int WEARLY = 6;
constexpr int WCPRE = 4;
constexpr int UWIN = 10;

__global__ __launch_bounds__(512, 2) void gmlp_kernel(const u16* __restrict__ x, const u16* __restrict__ w_in, const u16* __restrict__ w_sp,
                                                      const u16* __restrict__ b_sp, const u16* __restrict__ w_out, u16* __restrict__ out,
                                                      char* __restrict__ ws) {
#if defined(__HIP_DEVICE_COMPILE__)
    extern __shared__ __attribute__((aligned(16))) char shm_[];
    LAS char* const lds = (LAS char*)shm_;
    const unsigned lds0 = (unsigned)(size_t)lds;
    unsigned long long pc;
    unsigned code_len;
    asm volatile("s_getpc_b64 %0\n.Lgmlp_pc0:\n\ts_mov_b32 %1, .Lgmlp_end-.Lgmlp_pc0" : "=s"(pc), "=s"(code_len));
    const i32x4 scode = make_srd((const void*)pc, code_len);
    const int wid = __builtin_amdgcn_readfirstlane(threadIdx.x >> 6);
    const int xcd = blockIdx.x & 7, loc = blockIdx.x >> 3;
    const int il = loc & 7, jl = loc >> 3;
    const int pi = 8 * (xcd >> 2) + il, pj = 4 * (xcd & 3) + jl;
    if (blockIdx.x < 16 && wid < 4)
        touch(scode, (unsigned)(fresh_lane() + 64 * (wid + 4 * loc)) * 128u, 0, lds0 + SCRAP);
    asm volatile("" ::"s"(x), "s"(w_in), "s"(w_sp), "s"(w_out), "s"(ws));
    unsigned tag;
    auto get_tag = [&]() {
        asm volatile("s_load_dword %0, %1, 0x0\n\ts_waitcnt lgkmcnt(0)" : "=s"(tag) : "s"(ws) : "memory");
        tag += 1u;
    };
    const i32x4 swc = make_srd(ws, WS_END);
    const __amdgpu_buffer_rsrc_t rw = __builtin_amdgcn_make_buffer_rsrc((void*)ws, 0, WS_END, 0x00020000);
    u32x4 W[9][5];
    auto load_wo = [&](auto S0, auto S1) {
        const int lane = fresh_lane();
        const unsigned hl = (lane & 8) ? OOB >> 4 : (lane >> 4) * 8 + (lane & 7);
        const unsigned wb = WS_WO + ((pj & 7) * 16 + 2 * wid) * 20736;
#pragma unroll
        for (int s = S0.v; s < S1.v; ++s)
#pragma unroll
            for (int n = 0; n < 5; ++n) {
                const unsigned pb = wb + (s >> 2) * 20736;
                if (s < 8) W[s][n] = __builtin_amdgcn_raw_buffer_load_b128(rw, (n < 4 ? lane : hl) * 16, n < 4 ? pb + (s & 3) * 4096 + n * 1024 : pb + 18432 + (s & 3) * 512, 16);
                else {
                    const unsigned o = n < 4 ? wb + 16384 + n * 512 : wb + 20480;
                    const u32x2 t0 = __builtin_amdgcn_raw_buffer_load_b64(rw, (n < 4 ? lane : hl) * 8, o, 16);
                    const u32x2 t1 = __builtin_amdgcn_raw_buffer_load_b64(rw, (n < 4 ? lane : hl) * 8, o + 20736, 16);
                    W[s][n] = (u32x4){t0[0], t0[1], t1[0], t1[1]};
                }
            }
    };

    if (wid < 4) {
        const int w = wid;
        f32x4 acc[9][2];
        {
            unsigned aB, aM, aR, aRdy, aDone;
            unsigned seen = 0, val;
            unsigned long long lag = 0;
            {
                const int lane = fresh_lane(), q = lane >> 4, qq = (lane >> 2) & 3, pp = lane & 3;
                if (w == 0) *(LAS u32x4*)(lds + RDY + lane * 16) = (u32x4){0u, 0u, 0u, 0u};
                aB = addrB(lds0, w, lane);
                aM = lds0 + AP + qq * BLK + ((q & 1) + 4 * (q >> 1)) * 128 + pp * 8;
                aR = lds0 + AREM + (qq + 4 * (q & 1) + 16 * (q >> 1)) * 32 + pp * 8;
                aRdy = opaque(lds0 + RDY + lane * 4); aDone = opaque(lds0 + DONE + w * 64 + (lane & 15) * 4);
            }
            get_tag();
            {
                const int l = fresh_lane();
                touch(make_srd(w_sp, S * S * 2), l < 32 ? l * 128 : OOB, (128 * pi + 8 * pj + 2 * w) * (S * 2), lds0 + SCRAP);
            }
            s16x8 X[18][2];
            constexpr int WIN = 6;
            s16x4 Wf[WIN][2];
            auto pass = [&](auto PA) {
                constexpr bool first = PA.v == 0;
                constexpr int T0 = first ? 0 : 9;
                auto rd = [&](auto N, auto H) {
                    constexpr int n = N.v, h = H.v, kt = n / 18, ks = (n % 18) / 9, a = n % 9, T = T0 + kt;
                    if constexpr (n % 18 == 0 && h == 0) {
                        if constexpr (first) bar();
                        else if (kt == 0 || __builtin_expect(lag != 0, 0)) spin4(lds, RDY, fresh_lane(), T + 1);
                        SCHED0();
                    }
                    if constexpr (first && a == 0) X[2 * kt + ks][h] = lds_b128(lp(aB) + ks * 64 + h * 512);
                    if constexpr (a < 8) Wf[n % WIN][h] = lds_tr(lp(aM) + (a >> 2) * 8 * BLK + ks * 4 * BLK + (a & 3) * 32 + h * 256);
                    else Wf[n % WIN][h] = lds_tr(lp(aR) + ks * 1024 + h * 256);
                    SCHED0();
                    if constexpr (!first && n % 18 == 9 && h == 0 && kt < 8) seen = *(volatile LAS unsigned*)(size_t)aRdy;
                    if constexpr (!first && n % 18 == 17 && h == 0 && kt < 8) lag = lanes_below(seen, T + 2);
                    if constexpr (first && n % 18 == 10 && h == 0 && kt < 8) bump<(((T + 1) & 3) - (T & 3)) * SLOT>(aB);
                    if constexpr (n % 18 == 16 && h == 1 && T < 17) bump<g1_main(T + 1) - g1_main(T)>(aM);
                    if constexpr (n % 18 == 1 && h == 0 && T > 0) bump<g1_rem(T) - g1_rem(T - 1)>(aR);
                };
                sfor<0, WIN - 1>([&](auto N) { rd(N, IC<0>{}); rd(N, IC<1>{}); });
                sfor<0, 324>([&](auto M) {
                    constexpr int n = M.v >> 1, b = M.v & 1, kx = n / 9, a = n % 9;
                    const s16x8 wf = cat8(Wf[n % WIN][0], Wf[n % WIN][1]);
                    if constexpr (first) { if constexpr (kx == 0) mfma_new(acc[a][b], X[kx][b], wf); else mfma_acc(acc[a][b], X[kx][b], wf); }
                    else { if constexpr (kx == 0) mfma_new(acc[a][b], wf, X[kx][b]); else mfma_acc(acc[a][b], wf, X[kx][b]); }
                    SCHED0();
                    if constexpr (n + WIN - 1 < 162) rd(IC<n + WIN - 1>{}, IC<b>{});
                    if constexpr (!first && n % 18 == 16 && b == 1) val = opaque(T0 + n / 18 + 1);
                    if constexpr (!first && n % 18 == 17 && b == 0) *(volatile LAS unsigned*)(size_t)aDone = val;
                    SCHED0();
                });
                asm volatile("s_nop 7" ::: "memory");
                SCHED0();
            };
            waitl0();
            pass(IC<0>{});
            bar();
            {
                const int lane = fresh_lane();
                f32x2 s1[4] = {}, s2[4] = {};
#pragma unroll
                for (int a = 0; a < 9; ++a) {
#pragma unroll
                    for (int k = 0; k < 4; ++k) {
                        const f32x2 y = gelu2((f32x2){acc[a][k >> 1][2 * (k & 1)], acc[a][k >> 1][2 * (k & 1) + 1]});
                        s1[k] += y; s2[k] += y * y;
                        acc[a][k >> 1][2 * (k & 1)] = y.x; acc[a][k >> 1][2 * (k & 1) + 1] = y.y;
                    }
                    asm volatile("" : "+v"(acc[a][0]), "+v"(acc[a][1]));
                    SCHED0();
                }
                publish_stats(s1, s2, swc, WS_STATS + (pi * 16 + pj) * 2048, w, lane, tag);
#pragma unroll
                for (int a = 0; a < 9; ++a)
                    *(LAS u32x4*)stash_at(lds, w, lane, a) = (u32x4){pk_bf16(acc[a][0][0], acc[a][0][1]), pk_bf16(acc[a][0][2], acc[a][0][3]), pk_bf16(acc[a][1][0], acc[a][1][1]),
                                                                     pk_bf16(acc[a][1][2], acc[a][1][3])};
                waitl0();
                post(lds, HOFF, w, lane, 1);
            }
            pass(IC<1>{});
        }
        const int lane = fresh_lane(), c = lane & 15;
        float bias[2];
#pragma unroll
        for (int b = 0; b < 2; ++b) bias[b] = __uint_as_float((unsigned)b_sp[128 * pi + 32 * w + 8 * (c >> 2) + 4 * b + (c & 3)] << 16);
        u32x4 z1[9];
#pragma unroll
        for (int a = 0; a < 9; ++a) {
#pragma unroll
            for (int k = 0; k < 4; ++k) {
                const f32x2 y = gelu2((f32x2){acc[a][k >> 1][2 * (k & 1)], acc[a][k >> 1][2 * (k & 1) + 1]});
                z1[a][k] = pk_bf16(y.x, y.y);
            }
            asm volatile("" : "+v"(z1[a]));
        }
        const unsigned bB[2] = {opaque(addrB(lds0, w, lane)), opaque(addrB(lds0, w, lane) + 2 * SLOT)};
        const unsigned bA[4] = {opaque(lds0 + AP + lane * 16), opaque(lds0 + AP + lane * 16 + SLOT), opaque(lds0 + AP + lane * 16 + 2 * SLOT), opaque(lds0 + AP + lane * 16 + 3 * SLOT)};
        auto rdB = [&](int slot, int ks, int b) { return lds_b128(lp(bB[slot >> 1]) + (slot & 1) * SLOT + ks * 64 + b * 512); };
        auto rdA = [&](int slot, int ks, int a) { return lds_b128(lp(bA[slot]) + (ks * 9 + a) * 1024); };
        s16x8 A0[9], A1[9], B0[2], B1[2];
#pragma unroll
        for (int a = 0; a < 9; ++a) {
            acc[a][0] = (f32x4){bias[0], bias[0], bias[0], bias[0]}; acc[a][1] = (f32x4){bias[1], bias[1], bias[1], bias[1]};
            asm volatile("" : "+v"(acc[a][0]), "+v"(acc[a][1]));
        }
        auto step = [&](s16x8(&A)[9], s16x8(&B)[2], s16x8(&An)[9], s16x8(&Bn)[2], auto SO, auto KS) {
            sfor<0, 18>([&](auto I) {
                constexpr int i = I.v, a = i >> 1, b = i & 1;
                mfma_acc(acc[a][b], A[a], B[b]);
                SCHED0();
                if constexpr (i < 2) Bn[i] = rdB(SO.v, KS.v, i);
                else if constexpr (i < 11) An[i - 2] = rdA(SO.v, KS.v, i - 2);
                SCHED0();
            });
        };
        auto tile = [&](auto SL) {
            step(A0, B0, A1, B1, SL, IC<1>{});
            bar(); SCHED0();
            step(A1, B1, A0, B0, IC<(SL.v + 1) & 3>{}, IC<0>{});
        };
        SCHED0();
        bar();
        B0[0] = rdB(2, 0, 0); B0[1] = rdB(2, 0, 1);
#pragma unroll
        for (int a = 0; a < 9; ++a) A0[a] = rdA(2, 0, a);
        SCHED0();
        tile(IC<2>{}); tile(IC<3>{});
        for (int it = 0; it < 7; ++it) sfor<0, 4>(tile);
        bar();
        const s16x8 Bt[3][2] = {{rdB(0, 1, 0), rdB(0, 1, 1)}, {rdB(1, 0, 0), rdB(1, 0, 1)}, {rdB(1, 1, 0), rdB(1, 1, 1)}};
        auto rd3 = [&](auto AA) { A1[3 * (AA.v % 3)] = rdA(0, 1, AA.v); A1[3 * (AA.v % 3) + 1] = rdA(1, 0, AA.v); A1[3 * (AA.v % 3) + 2] = rdA(1, 1, AA.v); };
        sfor<0, 3>(rd3);
        SCHED0();
        const unsigned ub = WS_U + ((pi * 2 + (w >> 1)) * 16 + pj) * 18432 + (w & 1) * 2048;
        auto gate = [&](int a, int b) {
            const f32x2 u0 = (f32x2){acc[a][b][0], acc[a][b][1]} * (f32x2){bf_lo(z1[a][2 * b]), bf_hi(z1[a][2 * b])};
            const f32x2 u1 = (f32x2){acc[a][b][2], acc[a][b][3]} * (f32x2){bf_lo(z1[a][2 * b + 1]), bf_hi(z1[a][2 * b + 1])};
            return (u32x2){pk_bf16(u0.x, u0.y), pk_bf16(u1.x, u1.y)};
        };
        sfor<0, 9>([&](auto AA) {
            constexpr int a = AA.v;
            mfma_acc(acc[a][0], A0[a], B0[0]); mfma_acc(acc[a][1], A0[a], B0[1]);
#pragma unroll
            for (int k = 0; k < 3; ++k) { mfma_acc(acc[a][0], A1[3 * (a % 3) + k], Bt[k][0]); mfma_acc(acc[a][1], A1[3 * (a % 3) + k], Bt[k][1]); }
            SCHED0();
            if constexpr (a + 3 < 9) rd3(IC<a + 3>{});
            if constexpr ((a & 1) == 1 || a == 8) {
                asm volatile("s_nop 7" : "+v"(acc[a][0]), "+v"(acc[a][1]), "+v"(acc[a - 1][0]), "+v"(acc[a - 1][1]));
#pragma unroll
                for (int b = 0; b < 2; ++b) {
                    if constexpr (a < 8) {
                        const u32x2 lo = gate(a - 1, b), hi = gate(a, b);
                        st16_sc1(swc, lane * 16, ub + (a >> 1) * 4096 + b * 1024, (u32x4){lo[0], lo[1], hi[0], hi[1]});
                    } else st8_sc1(swc, lane * 8, ub - (w & 1) * 1024 + 16384 + b * 512, gate(8, b));
                }
            }
            SCHED0();
        });
        SCHED0();
        load_wo(IC<0>{}, IC<WCPRE>{});
        SCHED0();
        waitv<5 * WCPRE>();
        st4_sc1(swc, lane == 0 ? 0u : OOB, WS_UFLAG + (((pi * 2 + (w >> 1)) * 16 + pj) * 2 + (w & 1)) * 4, tag);
        post(lds, DONE, w, lane, 51);
        SCHED0();
        load_wo(IC<WCPRE>{}, IC<9>{});
        SCHED0();
    } else {
        __builtin_amdgcn_s_setprio(2);
        const int pw = wid - 4;
        const int lane = fresh_lane(), q = lane >> 4, c = lane & 15, pos = lane >> 3, ch = lane & 7;
        const unsigned lq = lds0 + 4 * pw * BLK;
        const unsigned rot = (il + 2 * jl + 4 * (xcd >> 2)) & 7;
        unsigned kn = 0, kc = 8;
        const unsigned rowB = 128 * pi + 32 * pw + 8 * (((pos >> 1) & 1) + 2 * (pos & 1)) + 4 * (pos >> 2);
        const i32x4 sx = make_srd(x, S * D * 2);
        const V4 vX = strided4(rowB * (D * 2) + ch * 16, D * 2);
        auto ld_x = [&](int slot) { dma16x4b(sx, vX, kc * 128, lq + slot * SLOT); };
        ld_x(0);
        SCHED0();
        const i32x4 swi = make_srd(w_in, D * F * 2), ssp = make_srd(w_sp, S * S * 2), swo = make_srd(w_out, FH * D * 2);
        const V4 vWm = strided4((32 * (pw & 1) + 8 * ((pos & 1) + 2 * (pos >> 2)) + 4 * ((pos >> 1) & 1)) * (F * 2) + ch * 16 + colmap0(pj) * 2 + (pw >> 1) * 128, F * 2);
        const int rho = 16 * (pw & 1) + ((lane >> 1) & 15);
        const unsigned vWr = (32 * (pw >> 1) + 8 * (((rho >> 2) & 1) + 2 * (rho >> 4)) + 4 * ((rho >> 3) & 1) + (rho & 3)) * (F * 2) + (lane & 1) * 16 + colmap8(pj) * 2;
        auto ld_w = [&](int half, unsigned at, unsigned atr) {
            const unsigned so = kc * 64 * (F * 2) + half * (FH * 2);
            dma16x4b(swi, vWm, so, lq + at);
            dma16_half(swi, vWr, so, lds0 + atr + pw * 512);
            kn = kn == 8 ? 0 : kn + 1;
            kc = kn == 0 ? 8 : (rot + kn) & 7;
            asm volatile("" : "+s"(kc), "+s"(kn));
        };
        auto ld_b = [&](auto TT) { ld_w(0, g1_main(TT.v), g1_rem(TT.v)); };
        auto landed = [&](int T) { post(lds, RDY, pw, lane, T + 1); };
        ld_w(1, AP, AREM);
#pragma nounroll
        for (int t = 1; t < 9; ++t) { ld_x(t & 3); ld_w(1, (t & 3) * SLOT + AP, (t & 3) * SLOT + AREM); waitv<9>(); bar(); }
        ld_b(IC<9>{}); waitv<5>(); bar();
        ld_b(IC<10>{});
        get_tag();
        bar();
        {
            const int g3 = pj & 7, hh = pj >> 3, pp = lane & 3, r4 = lane >> 2;
            const unsigned vm = pos * (D * 2) + ch * 16 + g3 * 128;
            const unsigned wb = WS_WO + (g3 * 16 + pi) * 20736, sg = 2 * SLOT + 4 * pw * BLK;
            const LAS char* const sp = lds + sg;
            const int p = 2 * hh + (pw & 1);
            if (pw < 2) {
                const unsigned f0 = colmap0(pi) + 32 * p;
                dma16x4s<1024, 8 * D * 2>(swo, vm, f0 * (D * 2), lds0 + sg);
                dma16_half(swo, (f0 + (lane & 31)) * (D * 2) + 1024 + g3 * 16, 0, lds0 + sg + SLOT);
            } else {
                const unsigned f8 = colmap8(pi);
                dma16(swo, vm, f8 * (D * 2), lds0 + sg);
                dma16(swo, vm, (f8 + 8) * (D * 2), lds0 + sg + 1024);
                if (p == 0) dma16_half(swo, (f8 + (lane & 15)) * (D * 2) + 1024 + g3 * 16, 0, lds0 + sg + SLOT);
            }
            ld_b(IC<11>{}); ld_b(IC<12>{});
            waitv<10>(); landed(10);
            if (pw < 2) {
                s16x8 fr[4];
#pragma unroll
                for (int n = 0; n < 4; ++n) {
                    const int co = r4 * 128 + (32 * (n >> 1) + 8 * pp + 4 * (n & 1)) * 2;
                    fr[n] = cat8(lds_tr(sp + co), lds_tr(sp + 2048 + co));
                }
                const s16x8 f4 = cat8(lds_tr(sp + SLOT + r4 * 16 + (pp & 1) * 8), lds_tr(sp + SLOT + (16 + r4) * 16 + (pp & 1) * 8));
#pragma unroll
                for (int n = 0; n < 4; ++n) st16_sc1(swc, lane * 16, wb + p * 4096 + n * 1024, __builtin_bit_cast(u32x4, fr[n]));
                st16_sc1(swc, c < 8 ? (unsigned)(q * 8 + c) * 16u : OOB, wb + 18432 + p * 512, __builtin_bit_cast(u32x4, f4));
            } else {
                const s16x4 h0 = lds_tr(sp + r4 * 128 + (32 * (p >> 1) + 8 * pp + 4 * (p & 1)) * 2);
                const s16x4 h1 = lds_tr(sp + SLOT + r4 * 16 + (pp & 1) * 8);
                st8_sc1(swc, lane * 8, wb + 16384 + p * 512, __builtin_bit_cast(u32x2, h0));
                st8_sc1(swc, p == 0 && c < 8 ? (unsigned)(q * 8 + c) * 8u : OOB, wb + 20480, __builtin_bit_cast(u32x2, h1));
            }
        }
        touch(ssp, lane >= 32 ? lane * 128 : OOB, (128 * pi + 8 * pj + 2 * pw) * (S * 2), lds0 + SCRAP);
        u32x2 wf;
        {
            u32x4 zz[9], gr[8];
            spin1(lds, HOFF, pw, 1);
#pragma unroll
            for (int n = 0; n < 9; ++n) zz[n] = *(const LAS u32x4*)stash_at(lds, pw, lane, n);
            waitv<1>(); landed(12);
            st4_sc1(swc, lane == 0 ? 0u : OOB, WS_WFLAG + ((pj & 7) * 128 + pi * 8 + (pj >> 3) * 4 + pw) * 4, tag);
            ld_b(IC<13>{}); ld_b(IC<14>{});
            waitl0();
            asm volatile("" : "+v"(zz[0]), "+v"(zz[1]), "+v"(zz[2]), "+v"(zz[3]), "+v"(zz[4]), "+v"(zz[5]), "+v"(zz[6]), "+v"(zz[7]), "+v"(zz[8]));
            const unsigned gvo = (c >> 3) * 2048 + (32 * pw + 8 * q + (c & 7)) * 16, gso = WS_STATS + pi * 16 * 2048;
            auto fetch = [&]() {
#pragma unroll
                for (int n = 0; n < 8; ++n) asm volatile("buffer_load_dwordx4 %0, %1, %2, %3 offen sc1" : "=v"(gr[n]) : "v"(gvo), "s"(swc), "s"(gso + n * 4096) : "memory");
                asm volatile("buffer_load_dwordx2 %0, %1, %2, %3 offen sc1" : "=v"(wf) : "v"(lane * 8), "s"(swc), "s"(WS_WFLAG + (pj & 7) * 512) : "memory");
            };
            auto ready = [&]() {
                asm volatile("" : "+v"(gr[0]), "+v"(gr[1]), "+v"(gr[2]), "+v"(gr[3]), "+v"(gr[4]), "+v"(gr[5]), "+v"(gr[6]), "+v"(gr[7]), "+v"(wf));
                unsigned bad = 0;
#pragma unroll
                for (int n = 0; n < 8; ++n) bad |= (gr[n][1] ^ tag) | (gr[n][3] ^ tag);
                return __all(bad == 0);
            };
            float zf[9][8];
#pragma unroll
            for (int a = 0; a < 9; ++a)
#pragma unroll
                for (int k = 0; k < 4; ++k) { zf[a][2 * k] = bf_lo(zz[a][k]); zf[a][2 * k + 1] = bf_hi(zz[a][k]); asm volatile("" : "+v"(zf[a][2 * k]), "+v"(zf[a][2 * k + 1])); }
            spin4(lds, DONE, lane, 10);
            fetch();
            ld_b(IC<15>{}); ld_b(IC<16>{});
            ld_b(IC<17>{});
            waitv<15>(); landed(14);
            if (!ready()) {
                const unsigned long long t0 = __builtin_amdgcn_s_memrealtime();
                for (;;) {
                    fetch(); waitv<0>(); landed(17);
                    if (ready() || __builtin_amdgcn_s_memrealtime() - t0 > SPIN_TICKS) break;
                }
            }
            auto tree = [&](int e) {
                float t[8];
#pragma unroll
                for (int n = 0; n < 8; ++n) t[n] = __uint_as_float(gr[n][e]);
                const float a0 = solo(t[0] + t[1]), a1 = solo(t[2] + t[3]), a2 = solo(t[4] + t[5]), a3 = solo(t[6] + t[7]);
                return dpp_add<0x128>(solo(solo(a0 + a1) + solo(a2 + a3)));
            };
            const float s1 = tree(0), s2 = tree(2);
            const float mean = solo(s1 * (1.0f / 2304.0f)), var = fmaxf(solo(solo(s2) * (1.0f / 2304.0f)) - mean * mean, 0.f);
            const float rinv = __builtin_amdgcn_rcpf(rbf(__builtin_amdgcn_sqrtf(rbf(rbf(var) + 1e-5f))));
            const float moff = -rbf(mean) * rinv;
            float rv[8], mv[8];
            sfor<0, 8>([&](auto K) {
                rv[K.v] = __int_as_float(__builtin_amdgcn_mov_dpp(__float_as_int(rinv), 0x150 + K.v, 0xf, 0xf, true));
                mv[K.v] = __int_as_float(__builtin_amdgcn_mov_dpp(__float_as_int(moff), 0x150 + K.v, 0xf, 0xf, true));
            });
            const unsigned zb = WS_Z2T + (pj * 64 + pi * 4 + pw) * 9216;
            waitv<0>(); landed(17);
#pragma unroll
            for (int a = 0; a < 9; ++a) {
                u32x4 o;
#pragma unroll
                for (int k = 0; k < 4; ++k)
                    o[k] = pk_bf16(solo(__builtin_fmaf(zf[a][2 * k], rv[2 * k], mv[2 * k])), solo(__builtin_fmaf(zf[a][2 * k + 1], rv[2 * k + 1], mv[2 * k + 1])));
                st16_sc1(swc, lane * 16, zb + a * 1024, o);
            }
        }
        const V4 vSP = strided4(rowB * (S * 2) + ch * 16, S * 2);
        auto ld_sp = [&](int kt, int slot) { dma16x4b(ssp, vSP, kt * 128, lq + slot * SLOT); };
        auto ld_z = [&](int kt, int slot) {
            const unsigned so = WS_Z2T + (pj * 32 + kt) * 18432;
            dma16x4_sc1(swc, lane * 16, so + pw * 4096, lds0 + slot * SLOT + AP + pw * 4096);
            dma16_half_sc1(swc, lane * 16, so + 16384 + pw * 512, lds0 + slot * SLOT + AP + 16384 + pw * 512);
        };
        waitv<0>();
        st4_sc1(swc, lane == 0 ? 0u : OOB, WS_ZFLAG + (pj * 64 + pi * 4 + pw) * 4, tag);
        spin4(lds, DONE, lane, 15); ld_sp(0, 2); ld_sp(1, 3);
        spin4(lds, DONE, lane, 17); ld_sp(2, 0); ld_sp(3, 1);
        if (!__all(wf[0] == tag && wf[1] == tag)) {
            poll_flags(swc, lane * 4, WS_WFLAG + (pj & 7) * 512, tag);
            poll_flags(swc, lane * 4, WS_WFLAG + (pj & 7) * 512 + 256, tag);
        }
        SCHED0();
        load_wo(IC<0>{}, IC<WEARLY>{});
        SCHED0();
        poll_flags(swc, lane * 4, WS_ZFLAG + pj * 256, tag);
        ld_z(0, 2); ld_z(1, 3); ld_z(2, 0);
        spin4(lds, DONE, lane, 18);
        ld_z(3, 1);
        waitv<15>(); bar();
        waitv<10>(); bar();
        waitv<5>(); bar();
        for (int t = 4; t < 32; ++t) { ld_z(t, (t + 2) & 3); ld_sp(t, (t + 2) & 3); waitv<9>(); bar(); }
        SCHED0();
        load_wo(IC<WEARLY>{}, IC<9>{});
        SCHED0();
        waitv<5 * (10 - WEARLY)>(); bar();
        __builtin_amdgcn_s_setprio(1);
    }

    {
        const int v = wid, h3 = pj >> 3, g3 = pj & 7;
        const int lane = fresh_lane(), q = lane >> 4, c = lane & 15;
        const unsigned ub = WS_U + ((pi * 2 + h3) * 16 + 2 * v) * 18432;
        spin4(lds, DONE, lane, 51);
        poll_flags(swc, (lane & 3) * 4, WS_UFLAG + ((pi * 2 + h3) * 16 + 2 * v) * 8, tag);
        SCHED0();
        constexpr int UW = UWIN;
        u32x4 U[UW];
        auto ldU = [&](auto G) {
            constexpr int r = G.v / 9, s = G.v % 9;
            if constexpr (s < 8) U[G.v % UW] = __builtin_amdgcn_raw_buffer_load_b128(rw, lane * 16, ub + (s >> 2) * 18432 + ((s & 3) * 4 + r) * 1024, 16);
            else {
                const u32x2 t0 = __builtin_amdgcn_raw_buffer_load_b64(rw, lane * 8, ub + 16384 + r * 512, 16);
                const u32x2 t1 = __builtin_amdgcn_raw_buffer_load_b64(rw, lane * 8, ub + 18432 + 16384 + r * 512, 16);
                U[G.v % UW] = (u32x4){t0[0], t0[1], t1[0], t1[1]};
            }
        };
        sfor<0, UW>(ldU);
        SCHED0();
        sfor<0, 4>([&](auto R) {
            constexpr int r = R.v;
            f32x4 o[5];
            sfor<0, 9>([&](auto SS) {
                constexpr int s = SS.v, g = 9 * r + s;
                const f32x4 z4 = {0.f, 0.f, 0.f, 0.f};
#pragma unroll
                for (int n = 0; n < 5; ++n) o[n] = mfma32(__builtin_bit_cast(s16x8, W[s][n]), __builtin_bit_cast(s16x8, U[g % UW]), s == 0 ? z4 : o[n]);
                SCHED0();
                if constexpr (g + UW < 36) ldU(IC<g + UW>{});
                SCHED0();
            });
            LAS char* const rb = lds + r * RED;
#pragma unroll
            for (int n = 0; n < 4; ++n) *(LAS f32x4*)(rb + (n * 8 + v) * 1024 + lane * 16) = o[n];
            if (lane < 32) *(LAS f32x4*)(rb + 32768 + v * 512 + lane * 16) = o[4];
            SCHED0();
        });
        waitl0(); bar();
        {
            const int r = v >> 1, m = v & 1;
            const LAS char* const rb = lds + r * RED;
            f32x4 t0[8], t1[8];
#pragma unroll
            for (int k = 0; k < 8; ++k) { t0[k] = *(const LAS f32x4*)(rb + (m * 16 + k) * 1024 + lane * 16); t1[k] = *(const LAS f32x4*)(rb + (m * 16 + 8 + k) * 1024 + lane * 16); }
            const f32x4 s0 = ((t0[0] + t0[1]) + (t0[2] + t0[3])) + ((t0[4] + t0[5]) + (t0[6] + t0[7]));
            const f32x4 s1 = ((t1[0] + t1[1]) + (t1[2] + t1[3])) + ((t1[4] + t1[5]) + (t1[6] + t1[7]));
            const int t = 128 * pi + 64 * h3 + 32 * (r >> 1) + 8 * (c >> 2) + 4 * (r & 1) + (c & 3);
            const __amdgpu_buffer_rsrc_t ro = __builtin_amdgcn_make_buffer_rsrc((void*)out, 0, S * D * 2, 0x00020000);
            __builtin_amdgcn_raw_buffer_store_b128((u32x4){pk_bf16(s0[0], s0[1]), pk_bf16(s0[2], s0[3]), pk_bf16(s1[0], s1[1]), pk_bf16(s1[2], s1[3])}, ro,
                                                   (t * D + 32 * m + 8 * q) * 2, g3 * 128, 16);
            if (m == 0) {
                f32x4 sh = (*(const LAS f32x4*)(rb + 32768 + lane * 16) + *(const LAS f32x4*)(rb + 33792 + lane * 16)) +
                           (*(const LAS f32x4*)(rb + 34816 + lane * 16) + *(const LAS f32x4*)(rb + 35840 + lane * 16));
#pragma unroll
                for (int k = 0; k < 4; ++k) sh[k] += __shfl_xor(sh[k], 32);
                if (lane < 32) __builtin_amdgcn_raw_buffer_store_b64((u32x2){pk_bf16(sh[0], sh[1]), pk_bf16(sh[2], sh[3])}, ro, (t * D + 4 * q) * 2, 1024 + g3 * 16, 16);
            }
        }
        if (blockIdx.x == 0 && v == 0 && lane == 0) *(unsigned*)(ws + WS_EPOCH) = tag;
        asm volatile(".Lgmlp_end:");
    }
#endif
}

extern "C" void kernel_launch(void* const* d_in, const int* in_sizes, int n_in, void* d_out, int out_size, void* d_ws, size_t ws_size,
                              hipStream_t stream) {
    (void)in_sizes; (void)n_in; (void)out_size; (void)ws_size;
    (void)hipFuncSetAttribute((const void*)gmlp_kernel, hipFuncAttributeMaxDynamicSharedMemorySize, LDS_BYTES);
    gmlp_kernel<<<256, 512, LDS_BYTES, stream>>>((const u16*)d_in[0], (const u16*)d_in[1], (const u16*)d_in[2], (const u16*)d_in[3],
                                                 (const u16*)d_in[4], (u16*)d_out, (char*)d_ws);
}
```
